```python
import torch

def _softmax(x, axis=-1):
    return torch.softmax(x, dim=axis)
def _gelu(x):
    return torch.nn.functional.gelu(x, approximate="tanh")
def _silu(x):
    return torch.nn.functional.silu(x)
def _sigmoid(x):
    return torch.sigmoid(x)
def _layernorm(x, axis=-1, eps=1e-5):
    m = x.mean(dim=axis, keepdim=True)
    v = x.var(dim=axis, keepdim=True, unbiased=False)
    return (x - m) / torch.sqrt(v + eps)
def _scatter_add(tgt, idx, src, axis=0):
    return tgt.clone().index_add_(axis, idx, src)
def _segment_sum(src, seg, n):
    out = torch.zeros((n,) + src.shape[1:], dtype=src.dtype)
    return out.index_add_(0, seg, src)
def _pad(x, pw):
    flat = tuple(v for pair in reversed(pw) for v in pair)
    return torch.nn.functional.pad(x, flat)

kernel_name = 'sequential_preLN__softmax_full_mha__moe_arch_mod__prompt'

def setup_inputs():

    _w = (lambda a: (lambda t: t.to(torch.bfloat16) if t.is_floating_point() else t)(torch.as_tensor(a)))
    def _A_setup():
        B, S, D = 1, 2048, 512
        return {
            "x": _w((lambda *s: torch.randn(*s, dtype=torch.bfloat16) * 0.05)(B, S, D)),
            "wqkv": _w((lambda *s: torch.randn(*s, dtype=torch.bfloat16) * 0.05)(D, 3 * D)),
            "wo": _w((lambda *s: torch.randn(*s, dtype=torch.bfloat16) * 0.05)(D, D)),
        }
    _A_in = _A_setup()

    def _B_setup():
        B, S, D, F, C = 1, 2048, 512, 1792, 2
        return {
            "x": _w((lambda *s: torch.randn(*s, dtype=torch.bfloat16) * 0.05)(B, S, D)),
            "w_r": _w((lambda *s: torch.randn(*s, dtype=torch.bfloat16) * 0.05)(D)),
            "w_gate": _w((lambda *s: torch.randn(*s, dtype=torch.bfloat16) * 0.05)(D, F)),
            "w_up": _w((lambda *s: torch.randn(*s, dtype=torch.bfloat16) * 0.05)(D, F)),
            "w_down": _w((lambda *s: torch.randn(*s, dtype=torch.bfloat16) * 0.05)(F, D)),
        }
    _B_in = _B_setup()

    return {
        "A_x": _A_in["x"],
        "A_wqkv": _A_in["wqkv"],
        "A_wo": _A_in["wo"],
        "B_x": _B_in["x"],
        "B_w_r": _B_in["w_r"],
        "B_w_gate": _B_in["w_gate"],
        "B_w_up": _B_in["w_up"],
        "B_w_down": _B_in["w_down"],
    }

def reference(A_x, A_wqkv, A_wo, B_x, B_w_r, B_w_gate, B_w_up, B_w_down):
    def _A_ref(x, wqkv, wo):
        B, S, D = x.shape
        H = 32; Dh = D // H
        qkv = (lambda x, *d: x.permute(*d))((x @ wqkv).reshape(B, S, 3, H, Dh), 2, 0, 3, 1, 4)
        q, k, v = qkv[0], qkv[1], qkv[2]
        scale = Dh ** -0.5
        scores = torch.einsum("bhqd,bhkd->bhqk", q, k) * scale
        p = _softmax(scores, axis=-1)
        o = torch.einsum("bhqk,bhkd->bhqd", p, v)
        return (lambda x, *d: x.permute(*d))(o, 0, 2, 1, 3).reshape(B, S, D) @ wo

    def _B_ref(x, w_r, w_gate, w_up, w_down):
        B, S, D = x.shape
        C = 2
        r = x @ w_r
        idx = torch.argsort(-r, axis=-1)[:, :C]
        idx_e = (lambda x, n, axis: torch.repeat_interleave(x, n, dim=axis))(idx.reshape(B, C, 1), D, axis=2)
        x_sel = (lambda x, i, axis: torch.take_along_dim(x, i, dim=axis))(x, idx_e, axis=1)
        g = _silu(x_sel @ w_gate)
        u = x_sel @ w_up
        y = (g * u) @ w_down
        r_sel = _sigmoid((lambda x, i, axis: torch.take_along_dim(x, i, dim=axis))(r, idx, axis=1))
        y = y * r_sel.reshape(B, C, 1)
        b_off = (torch.arange(B) * S).reshape(B, 1)
        flat_idx = (idx + b_off).reshape(B * C)
        zero = (x * 0.0).reshape(B * S, D)
        upd = _scatter_add(zero, flat_idx, y.reshape(B * C, D), axis=0)
        return x + upd.reshape(B, S, D)

    x = A_x
    h = x + _A_ref(_layernorm(x, axis=-1), A_wqkv, A_wo)
    h2 = h + _B_ref(_layernorm(h, axis=-1), B_w_r, B_w_gate, B_w_up, B_w_down)
    return h2
```

```cpp
#include <hip/hip_runtime.h>
#include <hip/hip_bf16.h>
#include <stdint.h>

typedef unsigned short u16;
typedef unsigned int u32;
typedef unsigned long long u64;
typedef __attribute__((ext_vector_type(8))) __bf16 bf16x8;
typedef __attribute__((ext_vector_type(2))) __bf16 bf16x2;
typedef __attribute__((ext_vector_type(4))) short s16x4;
typedef __attribute__((ext_vector_type(16))) float f32x16;
typedef __attribute__((ext_vector_type(4))) float f32x4;
typedef __attribute__((ext_vector_type(4))) u32 u32x4;
typedef __attribute__((ext_vector_type(2))) u32 u32x2;
#define LAS __attribute__((address_space(3)))
#define GAS __attribute__((address_space(1)))
typedef LAS const char* lds_cptr;
typedef LAS char* lds_ptr;

constexpr int S_ = 2048, D_ = 512, H_ = 32, F_ = 1792;
constexpr float LN_EPS = 1e-5f;
constexpr float QSCALE = 0.25f * 1.4426950408889634f;

constexpr size_t WS_Q = 0;
constexpr size_t WS_K = WS_Q + (2u << 20);
constexpr size_t WS_V = WS_K + (2u << 20);
constexpr size_t WS_O = WS_V + (2u << 20);
constexpr size_t WS_H = WS_O + (2u << 20);
constexpr size_t WS_ST = WS_H + (2u << 20);
constexpr size_t WS_GR = WS_ST + 3 * 2048 * 4;

#define MFMA32(a, b, c) __builtin_amdgcn_mfma_f32_32x32x16_bf16((a), (b), (c), 0, 0, 0)

__device__ __forceinline__ float bf2f(u32 lo16) { return __uint_as_float(lo16 << 16); }
__device__ __forceinline__ float bfhi2f(u32 w) { return __uint_as_float(w & 0xffff0000u); }
__device__ __forceinline__ float bflo2f(u32 w) { return __uint_as_float(w << 16); }
__device__ __forceinline__ u32 pk_bf16(float lo, float hi) {
    bf16x2 v; v[0] = (__bf16)lo; v[1] = (__bf16)hi; return __builtin_bit_cast(u32, v);
}
__device__ __forceinline__ float rbf(float x) { return (float)(__bf16)x; }

__device__ __forceinline__ void glds16(u32 voff, const void* sbase, u32 lds_dst) {
    u32 keep;
    asm volatile("s_mov_b32 %0, m0\n\ts_mov_b32 m0, %3\n\ts_nop 0\n\tglobal_load_lds_dwordx4 %1, %2\n\ts_mov_b32 m0, %0"
                 : "=&s"(keep) : "v"(voff), "s"(sbase), "s"(lds_dst) : "memory");
}
#define WAIT_BAR(N) asm volatile("s_waitcnt vmcnt(" #N ") lgkmcnt(0)\n\ts_barrier" ::: "memory")

__device__ __forceinline__ bf16x8 lds_frag(lds_cptr p) { return *(LAS const bf16x8*)p; }
__device__ __forceinline__ s16x4 lds_tr(lds_cptr p) {
    return __builtin_amdgcn_ds_read_tr16_b64_v4i16((LAS s16x4*)p);
}
__device__ __forceinline__ bf16x8 join_tr(s16x4 a, s16x4 b) {
    typedef __attribute__((ext_vector_type(8))) short s16x8;
    s16x8 r = {a[0], a[1], a[2], a[3], b[0], b[1], b[2], b[3]};
    return __builtin_bit_cast(bf16x8, r);
}
__device__ __forceinline__ float half_sum(float v) {
    auto r = __builtin_amdgcn_permlane32_swap(__float_as_uint(v), __float_as_uint(v), false, false);
    return __uint_as_float(r[0]) + __uint_as_float(r[1]);
}
__device__ __forceinline__ float half_max(float v) {
    auto r = __builtin_amdgcn_permlane32_swap(__float_as_uint(v), __float_as_uint(v), false, false);
    return fmaxf(__uint_as_float(r[0]), __uint_as_float(r[1]));
}
template <int CTRL> __device__ __forceinline__ float dpp_f(float v) {
    return __uint_as_float(__builtin_amdgcn_update_dpp(0, __float_as_uint(v), CTRL, 0xf, 0xf, true));
}
template <int CTRL> __device__ __forceinline__ u32 dpp_u(u32 v) {
    return __builtin_amdgcn_update_dpp(0, v, CTRL, 0xf, 0xf, true);
}
__device__ __forceinline__ float row16_sum(float v) {
    v += dpp_f<0xB1>(v);
    v += dpp_f<0x4E>(v);
    v += dpp_f<0x141>(v);
    v += dpp_f<0x140>(v);
    return v;
}
__device__ __forceinline__ float wave_sum(float v) {
    v = row16_sum(v);
    float a = __uint_as_float(__builtin_amdgcn_readlane(__float_as_uint(v), 0));
    float b = __uint_as_float(__builtin_amdgcn_readlane(__float_as_uint(v), 16));
    float c = __uint_as_float(__builtin_amdgcn_readlane(__float_as_uint(v), 32));
    float d = __uint_as_float(__builtin_amdgcn_readlane(__float_as_uint(v), 48));
    return (a + b) + (c + d);
}
__device__ __forceinline__ u32 wave_umax(u32 v) {
    v = max(v, dpp_u<0xB1>(v));
    v = max(v, dpp_u<0x4E>(v));
    v = max(v, dpp_u<0x141>(v));
    v = max(v, dpp_u<0x140>(v));
    u32 a = __builtin_amdgcn_readlane(v, 0), b = __builtin_amdgcn_readlane(v, 16);
    u32 c = __builtin_amdgcn_readlane(v, 32), d = __builtin_amdgcn_readlane(v, 48);
    return max(max(a, b), max(c, d));
}
__device__ __forceinline__ float dot2(u32 a, u32 b, float c) {
    return __builtin_amdgcn_fdot2_f32_bf16(__builtin_bit_cast(bf16x2, a), __builtin_bit_cast(bf16x2, b), c, false);
}

constexpr int K1_BUF = 32768;
__global__ __launch_bounds__(256, 1) void k1_qkv(const u16* __restrict__ x, const u16* __restrict__ wqkv,
                                                 char* __restrict__ ws) {
    extern __shared__ __attribute__((aligned(16))) char lds[];
    const int tid = threadIdx.x, lane = tid & 63;
    const int w = __builtin_amdgcn_readfirstlane(tid >> 6);
    const int cb = blockIdx.x & 7, rb = blockIdx.x >> 3;
    const u32 lds0 = (u32)(uintptr_t)lds;

    if (blockIdx.x < 10) {
        u32 i = blockIdx.x * 256 + tid;
        if (i < (3 * 2048 * 4 + F_ * 8) / 16) *(u32x4*)(ws + WS_ST + (size_t)i * 16) = u32x4{0, 0, 0, 0};
    }

    const int r8 = lane >> 3, c8 = lane & 7;
    const u32 xoff0 = (u32)(16 * w + r8) * 1024 + (u32)((c8 ^ ((lane >> 4) & 3)) * 16);
    const u32 xoff1 = (u32)(16 * w + 8 + r8) * 1024 + (u32)((c8 ^ (4 + ((lane >> 4) & 3))) * 16);
    const u32 woff = (u32)(16 * w + r8) * 3072 + (u32)((c8 ^ (((lane >> 4) & 1) << 2)) * 16);
    const char* xb = (const char*)x + (size_t)rb * 64 * 1024;
    const char* wb = (const char*)wqkv + cb * 128;
    auto issue = [&](int c) {
        const u32 dst = lds0 + (u32)(c & 3) * K1_BUF + (u32)w * 2048;
        glds16(xoff0, xb + c * 128, dst);
        glds16(xoff1, xb + c * 128, dst + 1024);
#pragma unroll
        for (int s = 0; s < 3; ++s) {
            glds16(woff, wb + (size_t)c * 64 * 3072 + s * 1024, dst + 8192 + s * 8192);
            glds16(woff, wb + (size_t)c * 64 * 3072 + s * 1024 + 8 * 3072, dst + 8192 + s * 8192 + 1024);
        }
    };
    issue(0); issue(1); issue(2); issue(3);

    const int rt = w & 1, ch = w >> 1;
    const int r32 = lane & 31, h = lane >> 5;
    const int xrow = 32 * rt + r32;
    const int xt = h ^ ((xrow >> 1) & 7);
    lds_cptr xp[4];
#pragma unroll
    for (int ks = 0; ks < 4; ++ks) xp[ks] = (lds_cptr)lds + xrow * 128 + ((xt ^ (2 * ks)) << 4);
    const int q4 = (lane & 15) >> 2, p4 = lane & 3, cg = (lane >> 4) & 1;
    const int wchunk = (4 * ch + 2 * cg + (p4 >> 1)) ^ ((q4 >> 1) << 2);
    const lds_cptr wp = (lds_cptr)lds + 8192 + (8 * h + q4) * 128 + wchunk * 16 + 8 * (p4 & 1);

    f32x16 aq = {}, ak = {}, av = {};
    float sx = 0.f, sxx = 0.f, cq = 0.f, ck = 0.f, cv = 0.f;
    const u32 ONES = 0x3f803f80u;

#pragma unroll
    for (int c = 0; c < 8; ++c) {
        if (c == 0) { WAIT_BAR(24); } else if (c <= 5) { WAIT_BAR(16); } else if (c == 6) { WAIT_BAR(8); } else { WAIT_BAR(0); }
        if (c >= 1 && c + 3 < 8) issue(c + 3);
        const int bo = (c & 3) * K1_BUF;
#pragma unroll
        for (int ks = 0; ks < 4; ++ks) {
            bf16x8 xf = lds_frag(xp[ks] + bo);
            bf16x8 wq = join_tr(lds_tr(wp + bo + ks * 2048), lds_tr(wp + bo + ks * 2048 + 512));
            bf16x8 wk = join_tr(lds_tr(wp + bo + 8192 + ks * 2048), lds_tr(wp + bo + 8192 + ks * 2048 + 512));
            bf16x8 wv = join_tr(lds_tr(wp + bo + 16384 + ks * 2048), lds_tr(wp + bo + 16384 + ks * 2048 + 512));
            aq = MFMA32(wq, xf, aq);
            ak = MFMA32(wk, xf, ak);
            av = MFMA32(xf, wv, av);
            u32x4 xu = __builtin_bit_cast(u32x4, xf), qu = __builtin_bit_cast(u32x4, wq);
            u32x4 ku = __builtin_bit_cast(u32x4, wk), vu = __builtin_bit_cast(u32x4, wv);
#pragma unroll
            for (int i = 0; i < 4; ++i) {
                sx = dot2(xu[i], ONES, sx); sxx = dot2(xu[i], xu[i], sxx);
                cq = dot2(qu[i], ONES, cq); ck = dot2(ku[i], ONES, ck); cv = dot2(vu[i], ONES, cv);
            }
        }
    }
    sx = half_sum(sx); sxx = half_sum(sxx); cq = half_sum(cq); ck = half_sum(ck); cv = half_sum(cv);
    const float mean = sx * (1.f / 512.f);
    float var = fmaxf(sxx * (1.f / 512.f) - mean * mean, 0.f);
    const float mb = rbf(mean);
    const float den = rbf(__builtin_sqrtf(rbf(rbf(var) + LN_EPS)));
    const float rden = 1.f / den;
    LAS float* sc = (LAS float*)((lds_ptr)lds + w * 1024);
    if (h == 0) { sc[r32] = mb; sc[32 + r32] = rden; } else { sc[64 + r32] = cq; sc[96 + r32] = ck; }
    asm volatile("s_waitcnt lgkmcnt(0)" ::: "memory");
    const int head0 = cb * 4 + ch * 2, tile = rb * 2 + rt;
    {
        u32 oq[8], ok[8];
        const float rq = rden * QSCALE;
#pragma unroll
        for (int g = 0; g < 4; ++g) {
            f32x4 c1 = *(LAS f32x4*)(sc + 64 + 8 * g + 4 * h);
            f32x4 c2 = *(LAS f32x4*)(sc + 96 + 8 * g + 4 * h);
            float a0 = (aq[4 * g + 0] - mb * c1[0]) * rq, a1 = (aq[4 * g + 1] - mb * c1[1]) * rq;
            float a2 = (aq[4 * g + 2] - mb * c1[2]) * rq, a3 = (aq[4 * g + 3] - mb * c1[3]) * rq;
            oq[2 * g] = pk_bf16(a0, a1); oq[2 * g + 1] = pk_bf16(a2, a3);
            float b0 = (ak[4 * g + 0] - mb * c2[0]) * rden, b1 = (ak[4 * g + 1] - mb * c2[1]) * rden;
            float b2 = (ak[4 * g + 2] - mb * c2[2]) * rden, b3 = (ak[4 * g + 3] - mb * c2[3]) * rden;
            ok[2 * g] = pk_bf16(b0, b1); ok[2 * g + 1] = pk_bf16(b2, b3);
        }
        const size_t o0 = ((size_t)(head0 * 64 + tile) * 64 + lane) * 16, o1 = o0 + 64 * 1024;
        *(u32x4*)(ws + WS_Q + o0) = u32x4{oq[0], oq[1], oq[2], oq[3]};
        *(u32x4*)(ws + WS_Q + o1) = u32x4{oq[4], oq[5], oq[6], oq[7]};
        *(u32x4*)(ws + WS_K + o0) = u32x4{ok[0], ok[1], ok[2], ok[3]};
        *(u32x4*)(ws + WS_K + o1) = u32x4{ok[4], ok[5], ok[6], ok[7]};
    }
    {
        u32 ov[8];
#pragma unroll
        for (int g = 0; g < 4; ++g) {
            f32x4 mm = *(LAS f32x4*)(sc + 8 * g + 4 * h);
            f32x4 rr = *(LAS f32x4*)(sc + 32 + 8 * g + 4 * h);
            float a0 = (av[4 * g + 0] - mm[0] * cv) * rr[0], a1 = (av[4 * g + 1] - mm[1] * cv) * rr[1];
            float a2 = (av[4 * g + 2] - mm[2] * cv) * rr[2], a3 = (av[4 * g + 3] - mm[3] * cv) * rr[3];
            ov[2 * g] = pk_bf16(a0, a1); ov[2 * g + 1] = pk_bf16(a2, a3);
        }
        const size_t o = (size_t)((head0 + (r32 >> 4)) * 64 + tile) * 1024 + h * 256 + (r32 & 15) * 16;
        *(u32x4*)(ws + WS_V + o) = u32x4{ov[0], ov[1], ov[2], ov[3]};
        *(u32x4*)(ws + WS_V + o + 512) = u32x4{ov[4], ov[5], ov[6], ov[7]};
    }
}

constexpr int K2_VSTRIDE = 1056;
constexpr int K2_VOFF = 65536;
constexpr int K2_LDS = K2_VOFF + 64 * K2_VSTRIDE;

__device__ __forceinline__ float max16(const f32x16& s) {
    float a = fmaxf(fmaxf(s[0], s[1]), s[2]), b = fmaxf(fmaxf(s[3], s[4]), s[5]);
    a = fmaxf(fmaxf(a, s[6]), s[7]); b = fmaxf(fmaxf(b, s[8]), s[9]);
    a = fmaxf(fmaxf(a, s[10]), s[11]); b = fmaxf(fmaxf(b, s[12]), s[13]);
    a = fmaxf(fmaxf(a, s[14]), s[15]);
    return fmaxf(a, b);
}
__device__ __forceinline__ void exp_pack(const f32x16& s, u32x4& p0, u32x4& p1) {
#pragma unroll
    for (int i = 0; i < 4; ++i) {
        p0[i] = pk_bf16(__builtin_amdgcn_exp2f(s[2 * i]), __builtin_amdgcn_exp2f(s[2 * i + 1]));
        p1[i] = pk_bf16(__builtin_amdgcn_exp2f(s[8 + 2 * i]), __builtin_amdgcn_exp2f(s[8 + 2 * i + 1]));
    }
}

__global__ __launch_bounds__(512, 2) void k2_attn(char* __restrict__ ws) {
    extern __shared__ __attribute__((aligned(16))) char lds[];
    const int tid = threadIdx.x, lane = tid & 63;
    const int w = __builtin_amdgcn_readfirstlane(tid >> 6);
    const int b = blockIdx.x;
    const int head = 4 * (b & 7) + ((b >> 3) & 3), qt = (b >> 5) * 8 + w;
    const u32 lds0 = (u32)(uintptr_t)lds;
    const int r32 = lane & 31, h = lane >> 5;

    u32x4 qraw;
    {
        const char* qp = ws + WS_Q + ((size_t)(head * 64 + qt) * 64 + lane) * 16;
        asm volatile("global_load_dwordx4 %0, %1, off" : "=v"(qraw) : "v"(qp) : "memory");
    }
    const char* kb = ws + WS_K + (size_t)head * 65536;
    const char* vb = ws + WS_V + (size_t)head * 65536;
    const u32 loff = (u32)lane * 16;
#pragma unroll
    for (int c = 0; c < 8; ++c) {
        const int t = 8 * c + w;
        glds16(loff, kb + t * 1024, lds0 + (u32)t * 1024);
        glds16(loff, vb + t * 1024, lds0 + K2_VOFF + (u32)t * K2_VSTRIDE);
    }
    if (tid < 64) *(LAS u32x4*)((lds_ptr)lds + K2_VOFF + tid * K2_VSTRIDE + 1024) = u32x4{0x3f803f80u, 0x3f803f80u, 0x3f803f80u, 0x3f803f80u};

    const lds_cptr kp = (lds_cptr)lds + lane * 16;
    const lds_cptr vp0 = (lds_cptr)lds + K2_VOFF + (r32 < 16 ? h * 256 + r32 * 16 : 1024);
    const lds_cptr vp1 = (lds_cptr)lds + K2_VOFF + (r32 < 16 ? 512 + h * 256 + r32 * 16 : 1024);

    asm volatile("s_waitcnt vmcnt(14) lgkmcnt(0)\n\ts_barrier" : "+v"(qraw) :: "memory");
    const bf16x8 qf = __builtin_bit_cast(bf16x8, qraw);

    f32x16 negm;
    {
        f32x16 z = {};
        f32x16 s0 = MFMA32(lds_frag(kp), qf, z);
        const float m0 = half_max(max16(s0));
#pragma unroll
        for (int i = 0; i < 16; ++i) negm[i] = -m0;
    }
    f32x16 o = {};
    f32x16 sc = MFMA32(lds_frag(kp), qf, negm);
#pragma unroll 1
    for (int c = 0; c < 8; ++c) {
#pragma unroll
        for (int i = 0; i < 8; ++i) {
            const int t = 8 * c + i;
            bf16x8 v0 = lds_frag(vp0 + t * K2_VSTRIDE), v1 = lds_frag(vp1 + t * K2_VSTRIDE);
            u32x4 p0, p1;
            exp_pack(sc, p0, p1);
            if (i == 7 && c < 2) {
                o = MFMA32(v0, __builtin_bit_cast(bf16x8, p0), o);
                o = MFMA32(v1, __builtin_bit_cast(bf16x8, p1), o);
                if (c == 0) { WAIT_BAR(12); } else { WAIT_BAR(0); }
                sc = MFMA32(lds_frag(kp + (t + 1) * 1024), qf, negm);
            } else {
                const int tn = (t + 1) & 63;
                sc = MFMA32(lds_frag(kp + tn * 1024), qf, negm);
                o = MFMA32(v0, __builtin_bit_cast(bf16x8, p0), o);
                o = MFMA32(v1, __builtin_bit_cast(bf16x8, p1), o);
            }
        }
    }
    float l = o[8];
    bool bad = !(l < 1.0e18f);
#pragma unroll
    for (int i = 0; i < 8; ++i) bad |= !(fabsf(o[i]) < 3.0e38f);
    if (__any(bad)) {
        float m = -INFINITY;
        f32x16 z = {};
        o = z;
#pragma unroll 1
        for (int t = 0; t < 64; ++t) {
            f32x16 s = MFMA32(lds_frag(kp + t * 1024), qf, z);
            const float mn = fmaxf(m, half_max(max16(s)));
            const float alpha = __builtin_amdgcn_exp2f(m - mn);
            m = mn;
#pragma unroll
            for (int i = 0; i < 16; ++i) { o[i] *= alpha; s[i] -= mn; }
            u32x4 p0, p1;
            exp_pack(s, p0, p1);
            o = MFMA32(lds_frag(vp0 + t * K2_VSTRIDE), __builtin_bit_cast(bf16x8, p0), o);
            o = MFMA32(lds_frag(vp1 + t * K2_VSTRIDE), __builtin_bit_cast(bf16x8, p1), o);
        }
        l = o[8];
    }
    const float rl = 1.f / l;
    u32x4 ov = {pk_bf16(o[0] * rl, o[1] * rl), pk_bf16(o[2] * rl, o[3] * rl),
                pk_bf16(o[4] * rl, o[5] * rl), pk_bf16(o[6] * rl, o[7] * rl)};
    *(u32x4*)(ws + WS_O + ((size_t)(qt * 32 + head) * 64 + lane) * 16) = ov;
}

constexpr int K3_LDS = 131072;
__global__ __launch_bounds__(256, 1) void k3_oproj(const u16* __restrict__ x, const u16* __restrict__ wo,
                                                   const u16* __restrict__ wr, char* __restrict__ ws) {
    extern __shared__ __attribute__((aligned(16))) char lds[];
    const int tid = threadIdx.x, lane = tid & 63;
    const int w = __builtin_amdgcn_readfirstlane(tid >> 6);
    const int b = blockIdx.x;
    const int rb = 4 * (b & 7) + ((b >> 3) & 3), cb = b >> 5;
    const u32 lds0 = (u32)(uintptr_t)lds;
    const int rt = w & 1, ct = w >> 1, r32 = lane & 31, h = lane >> 5;

    const int row = rb * 64 + rt * 32 + r32, col0 = cb * 64 + ct * 32 + 4 * h;
    u32x2 xr[4], wv[4];
#pragma unroll
    for (int g = 0; g < 4; ++g) {
        xr[g] = *(const u32x2*)(x + (size_t)row * 512 + col0 + 8 * g);
        wv[g] = *(const u32x2*)(wr + col0 + 8 * g);
    }

    const u32 aoff = (u32)lane * 16;
    const int r8 = lane >> 3, c8 = lane & 7;
    const u32 boff = (u32)r8 * 1024 + (u32)((c8 ^ (((lane >> 4) & 1) << 2)) * 16);
    const char* ab = ws + WS_O + (size_t)(rb * 2) * 32768;
    const char* bb = (const char*)wo + cb * 128;
#pragma unroll
    for (int c = 0; c < 4; ++c) {
#pragma unroll
        for (int i = 0; i < 4; ++i) {
            const int pa = 4 * w + i;
            const int ao = (pa >> 3) * 32768 + (8 * c + (pa & 7)) * 1024;
            glds16(aoff, ab + ao, lds0 + (u32)ao);
            const int pb = 16 * c + 4 * w + i;
            glds16(boff, bb + (size_t)pb * 8192, lds0 + 65536 + (u32)pb * 1024);
        }
    }

    const lds_cptr ap = (lds_cptr)lds + rt * 32768 + lane * 16;
    const int q4 = (lane & 15) >> 2, p4 = lane & 3, cg = (lane >> 4) & 1;
    const int wchunk = (4 * ct + 2 * cg + (p4 >> 1)) ^ ((q4 >> 1) << 2);
    const lds_cptr wp = (lds_cptr)lds + 65536 + (4 * h + q4) * 128 + wchunk * 16 + 8 * (p4 & 1);

    f32x16 acc = {};
#pragma unroll
    for (int c = 0; c < 4; ++c) {
        if (c == 0) { WAIT_BAR(24); } else if (c == 1) { WAIT_BAR(16); } else if (c == 2) { WAIT_BAR(8); } else { WAIT_BAR(0); }
#pragma unroll
        for (int i = 0; i < 8; ++i) {
            const int hd = 8 * c + i;
            bf16x8 of = lds_frag(ap + hd * 1024);
            bf16x8 wf = join_tr(lds_tr(wp + hd * 2048), lds_tr(wp + hd * 2048 + 1024));
            acc = MFMA32(wf, of, acc);
        }
    }
    float s1 = 0.f, s2 = 0.f, s3 = 0.f;
    u16* hp = (u16*)(ws + WS_H) + (size_t)row * 512 + col0;
#pragma unroll
    for (int g = 0; g < 4; ++g) {
        float h0 = rbf(bflo2f(xr[g][0]) + rbf(acc[4 * g + 0]));
        float h1 = rbf(bfhi2f(xr[g][0]) + rbf(acc[4 * g + 1]));
        float h2 = rbf(bflo2f(xr[g][1]) + rbf(acc[4 * g + 2]));
        float h3 = rbf(bfhi2f(xr[g][1]) + rbf(acc[4 * g + 3]));
        s1 += (h0 + h1) + (h2 + h3);
        s2 += (h0 * h0 + h1 * h1) + (h2 * h2 + h3 * h3);
        s3 += (h0 * bflo2f(wv[g][0]) + h1 * bfhi2f(wv[g][0])) + (h2 * bflo2f(wv[g][1]) + h3 * bfhi2f(wv[g][1]));
        *(u32x2*)(hp + 8 * g) = u32x2{pk_bf16(h0, h1), pk_bf16(h2, h3)};
    }
    s1 = half_sum(s1); s2 = half_sum(s2); s3 = half_sum(s3);
    float* st = (float*)(ws + WS_ST);
    if (h == 0) {
        atomicAdd(st + row, s1);
        atomicAdd(st + 2048 + row, s2);
    } else {
        atomicAdd(st + 4096 + row, s3);
    }
}

struct LnRow { float y[8]; float hv[8]; };
__device__ __forceinline__ void ln_row(const u32x4& raw, LnRow& o) {
#pragma unroll
    for (int i = 0; i < 4; ++i) { o.hv[2 * i] = bflo2f(raw[i]); o.hv[2 * i + 1] = bfhi2f(raw[i]); }
    float s = ((o.hv[0] + o.hv[1]) + (o.hv[2] + o.hv[3])) + ((o.hv[4] + o.hv[5]) + (o.hv[6] + o.hv[7]));
    const float mean = wave_sum(s) * (1.f / 512.f);
    float q = 0.f;
#pragma unroll
    for (int i = 0; i < 8; ++i) { float d = o.hv[i] - mean; q += d * d; }
    const float var = wave_sum(q) * (1.f / 512.f);
    const float mb = rbf(mean);
    const float den = rbf(__builtin_sqrtf(rbf(rbf(var) + LN_EPS)));
#pragma unroll
    for (int i = 0; i < 8; ++i) o.y[i] = rbf(rbf(o.hv[i] - mb) / den);
}
__device__ __forceinline__ u32 route_key(float r, u32 idx) {
    u32 u = __float_as_uint(r);
    u = (u & 0x80000000u) ? ~u : (u | 0x80000000u);
    return (u & 0xfffff800u) | (2047u - idx);
}
template <int N> __device__ __forceinline__ void wave_reduce_scatter(float* v) {
#pragma unroll
    for (int i = 0; i < N / 2; ++i) {
        auto r = __builtin_amdgcn_permlane32_swap(__float_as_uint(v[i]), __float_as_uint(v[i + N / 2]), false, false);
        v[i] = __uint_as_float(r[0]) + __uint_as_float(r[1]);
    }
#pragma unroll
    for (int i = 0; i < N / 4; ++i) {
        auto r = __builtin_amdgcn_permlane16_swap(__float_as_uint(v[i]), __float_as_uint(v[i + N / 4]), false, false);
        v[i] = __uint_as_float(r[0]) + __uint_as_float(r[1]);
    }
#pragma unroll
    for (int i = 0; i < N / 4; ++i) v[i] = row16_sum(v[i]);
}

constexpr int K4_NA = F_ / 8;
constexpr int K4_B0 = 192;
__global__ __launch_bounds__(256) void k4_tail(const u16* __restrict__ wr, const u16* __restrict__ wg,
                                               const u16* __restrict__ wu, const u16* __restrict__ wd,
                                               char* __restrict__ ws, u16* __restrict__ out) {
    __shared__ __attribute__((aligned(16))) float sm[320];
    const int tid = threadIdx.x, lane = tid & 63;
    const int w = __builtin_amdgcn_readfirstlane(tid >> 6);
    const int b = blockIdx.x;
    const int ja = (b & 7) * 28 + (b >> 3);
    const int jb = ((b - K4_B0) & 7) * 8 + ((b - K4_B0) >> 3);
    const bool isA = b < K4_NA, isB = b >= K4_B0;
    const u16* hbuf = (const u16*)(ws + WS_H);

    const float* st = (const float*)(ws + WS_ST);
    f32x4 s1a = *(const f32x4*)(st + tid * 8), s1b = *(const f32x4*)(st + tid * 8 + 4);
    f32x4 s2a = *(const f32x4*)(st + 2048 + tid * 8), s2b = *(const f32x4*)(st + 2048 + tid * 8 + 4);
    f32x4 s3a = *(const f32x4*)(st + 4096 + tid * 8), s3b = *(const f32x4*)(st + 4096 + tid * 8 + 4);
    const u32x4 wrv = *(const u32x4*)(wr + lane * 8);
    const int row0 = b * 8 + 2 * w;
    const u32x4 own0 = *(const u32x4*)(hbuf + (size_t)row0 * 512 + lane * 8);
    const u32x4 own1 = *(const u32x4*)(hbuf + (size_t)(row0 + 1) * 512 + lane * 8);
    u32x4 g0 = {}, g1 = {}, u0 = {}, u1 = {};
    if (isA) {
        const size_t o = (size_t)(8 * lane + 2 * w) * F_ + ja * 8;
        g0 = *(const u32x4*)(wg + o); g1 = *(const u32x4*)(wg + o + F_);
        u0 = *(const u32x4*)(wu + o); u1 = *(const u32x4*)(wu + o + F_);
    }
    u32x4 dn[7];
#pragma unroll
    for (int i = 0; i < 7; ++i) dn[i] = u32x4{};
    if (isB) {
#pragma unroll
        for (int i = 0; i < 7; ++i) dn[i] = *(const u32x4*)(wd + (size_t)(tid + 256 * i) * 512 + jb * 8);
    }

    float wrf[8];
#pragma unroll
    for (int i = 0; i < 4; ++i) { wrf[2 * i] = bflo2f(wrv[i]); wrf[2 * i + 1] = bfhi2f(wrv[i]); }
    const float wsum = wave_sum(((wrf[0] + wrf[1]) + (wrf[2] + wrf[3])) + ((wrf[4] + wrf[5]) + (wrf[6] + wrf[7])));
    u32 k1 = 0, k2 = 0;
#pragma unroll
    for (int i = 0; i < 8; ++i) {
        const float a1 = i < 4 ? s1a[i & 3] : s1b[i & 3], a2 = i < 4 ? s2a[i & 3] : s2b[i & 3], a3 = i < 4 ? s3a[i & 3] : s3b[i & 3];
        const float mean = a1 * (1.f / 512.f);
        const float var = fmaxf(a2 * (1.f / 512.f) - mean * mean, 0.f);
        const float r = (a3 - mean * wsum) * __builtin_amdgcn_rsqf(var + LN_EPS);
        const u32 key = route_key(r, (u32)(tid * 8 + i));
        k2 = max(k2, min(k1, key)); k1 = max(k1, key);
    }
    {
        const u32 t1 = wave_umax(k1);
        const u32 t2 = wave_umax(k1 == t1 ? k2 : k1);
        if (lane == 0) { ((u32*)sm)[2 * w] = t1; ((u32*)sm)[2 * w + 1] = t2; }
    }
    __syncthreads();
    u32 t1 = 0, t2 = 0;
#pragma unroll
    for (int i = 0; i < 8; ++i) { const u32 key = ((u32*)sm)[i]; t2 = max(t2, min(t1, key)); t1 = max(t1, key); }
    const int i1 = __builtin_amdgcn_readfirstlane(2047 - (int)(t1 & 2047u));
    const int i2 = __builtin_amdgcn_readfirstlane(2047 - (int)(t2 & 2047u));
    const u32x4 sel0 = *(const u32x4*)(hbuf + (size_t)i1 * 512 + lane * 8);
    const u32x4 sel1 = *(const u32x4*)(hbuf + (size_t)i2 * 512 + lane * 8);

    {
        LnRow a, c;
        ln_row(own0, a); ln_row(own1, c);
        u32x4 oa, oc;
#pragma unroll
        for (int i = 0; i < 4; ++i) {
            oa[i] = pk_bf16(a.hv[2 * i] + a.y[2 * i], a.hv[2 * i + 1] + a.y[2 * i + 1]);
            oc[i] = pk_bf16(c.hv[2 * i] + c.y[2 * i], c.hv[2 * i + 1] + c.y[2 * i + 1]);
        }
        if (row0 != i1 && row0 != i2) *(u32x4*)(out + (size_t)row0 * 512 + lane * 8) = oa;
        if (row0 + 1 != i1 && row0 + 1 != i2) *(u32x4*)(out + (size_t)(row0 + 1) * 512 + lane * 8) = oc;
    }
    LnRow ya, yb;
    ln_row(sel0, ya); ln_row(sel1, yb);
    GAS u64* gran = (GAS u64*)(ws + WS_GR);
    if (isA) {
        float y00 = 0.f, y01 = 0.f, y10 = 0.f, y11 = 0.f;
#pragma unroll
        for (int i = 0; i < 4; ++i) if (w == i) { y00 = ya.y[2 * i]; y01 = ya.y[2 * i + 1]; y10 = yb.y[2 * i]; y11 = yb.y[2 * i + 1]; }
        float v[32];
#pragma unroll
        for (int i = 0; i < 4; ++i) {
            const float ga = bflo2f(g0[i]), gb = bfhi2f(g0[i]), gc = bflo2f(g1[i]), gd = bfhi2f(g1[i]);
            const float ua = bflo2f(u0[i]), ub = bfhi2f(u0[i]), uc = bflo2f(u1[i]), ud = bfhi2f(u1[i]);
            v[2 * i] = y00 * ga + y01 * gc;      v[2 * i + 1] = y00 * gb + y01 * gd;
            v[8 + 2 * i] = y10 * ga + y11 * gc;  v[8 + 2 * i + 1] = y10 * gb + y11 * gd;
            v[16 + 2 * i] = y00 * ua + y01 * uc; v[16 + 2 * i + 1] = y00 * ub + y01 * ud;
            v[24 + 2 * i] = y10 * ua + y11 * uc; v[24 + 2 * i + 1] = y10 * ub + y11 * ud;
        }
        wave_reduce_scatter<32>(v);
        if ((lane & 15) == 0) {
            *(f32x4*)(sm + 64 + w * 32 + (lane >> 4) * 8) = f32x4{v[0], v[1], v[2], v[3]};
            *(f32x4*)(sm + 64 + w * 32 + (lane >> 4) * 8 + 4) = f32x4{v[4], v[5], v[6], v[7]};
        }
        __syncthreads();
        if (tid < 8) {
            float t[4];
#pragma unroll
            for (int j = 0; j < 4; ++j) t[j] = (sm[64 + 8 * j + tid] + sm[96 + 8 * j + tid]) + (sm[128 + 8 * j + tid] + sm[160 + 8 * j + tid]);
            float a[2];
#pragma unroll
            for (int k = 0; k < 2; ++k) {
                const float g = rbf(t[k]), u = rbf(t[2 + k]);
                const float sg = rbf(g / (1.f + __expf(-g)));
                a[k] = sg * u;
            }
            __hip_atomic_store(gran + ja * 8 + tid, (1ull << 32) | pk_bf16(a[0], a[1]), __ATOMIC_RELAXED, __HIP_MEMORY_SCOPE_AGENT);
        }
    }
    if (isB) {
        float ra = 0.f, rb_ = 0.f;
#pragma unroll
        for (int i = 0; i < 8; ++i) { ra += ya.y[i] * wrf[i]; rb_ += yb.y[i] * wrf[i]; }
        ra = rbf(wave_sum(ra)); rb_ = rbf(wave_sum(rb_));
        if (w == 0 && lane == jb) {
#pragma unroll
            for (int i = 0; i < 8; ++i) { sm[192 + i] = ya.hv[i]; sm[200 + i] = yb.hv[i]; sm[208 + i] = ya.y[i]; sm[216 + i] = yb.y[i]; }
        }
        u32 av[7];
        for (u32 spins = 0; spins < (1u << 22); ++spins) {
            bool ok = true;
#pragma unroll
            for (int i = 0; i < 7; ++i) {
                const u64 gq = __hip_atomic_load(gran + tid + 256 * i, __ATOMIC_RELAXED, __HIP_MEMORY_SCOPE_AGENT);
                av[i] = (u32)gq; ok &= (gq >> 32) == 1ull;
            }
            if (__all(ok)) break;
            __builtin_amdgcn_s_sleep(1);
        }
        float v[16];
#pragma unroll
        for (int i = 0; i < 16; ++i) v[i] = 0.f;
#pragma unroll
        for (int i = 0; i < 7; ++i) {
            const float a0 = bflo2f(av[i]), a1 = bfhi2f(av[i]);
#pragma unroll
            for (int c = 0; c < 4; ++c) {
                const float d0 = bflo2f(dn[i][c]), d1 = bfhi2f(dn[i][c]);
                v[2 * c] += a0 * d0; v[2 * c + 1] += a0 * d1;
                v[8 + 2 * c] += a1 * d0; v[8 + 2 * c + 1] += a1 * d1;
            }
        }
        wave_reduce_scatter<16>(v);
        if ((lane & 15) == 0) *(f32x4*)(sm + 224 + w * 16 + (lane >> 4) * 4) = f32x4{v[0], v[1], v[2], v[3]};
        __syncthreads();
        if (tid < 16) {
            const float ysum = rbf((sm[224 + tid] + sm[240 + tid]) + (sm[256 + tid] + sm[272 + tid]));
            const float rr = tid < 8 ? ra : rb_;
            const float sig = rbf(1.f / (1.f + __expf(-rr)));
            const float upd = rbf(ysum * sig);
            const float hv = sm[192 + tid], yv = sm[208 + tid];
            const float res = hv + rbf(yv + upd);
            const int orow = tid < 8 ? i1 : i2;
            out[(size_t)orow * 512 + jb * 8 + (tid & 7)] = (u16)(pk_bf16(res, 0.f) & 0xffffu);
        }
    }
}

extern "C" void kernel_launch(void* const* d_in, const int* in_sizes, int n_in,
                              void* d_out, int out_size, void* d_ws, size_t ws_size,
                              hipStream_t stream) {
    const u16* x = (const u16*)d_in[0];
    const u16* wqkv = (const u16*)d_in[1];
    const u16* wo = (const u16*)d_in[2];
    const u16* wr = (const u16*)d_in[4];
    const u16* wg = (const u16*)d_in[5];
    const u16* wu = (const u16*)d_in[6];
    const u16* wd = (const u16*)d_in[7];
    char* ws = (char*)d_ws;
    (void)hipFuncSetAttribute((const void*)k1_qkv, hipFuncAttributeMaxDynamicSharedMemorySize, 4 * K1_BUF);
    (void)hipFuncSetAttribute((const void*)k2_attn, hipFuncAttributeMaxDynamicSharedMemorySize, K2_LDS);
    (void)hipFuncSetAttribute((const void*)k3_oproj, hipFuncAttributeMaxDynamicSharedMemorySize, K3_LDS);
    k1_qkv<<<256, 256, 4 * K1_BUF, stream>>>(x, wqkv, ws);
    k2_attn<<<256, 512, K2_LDS, stream>>>(ws);
    k3_oproj<<<256, 256, K3_LDS, stream>>>(x, wo, wr, ws);
    k4_tail<<<256, 256, 0, stream>>>(wr, wg, wu, wd, ws, (u16*)d_out);
}
```

```cpp
#include <hip/hip_runtime.h>
#include <hip/hip_bf16.h>
#include <stdint.h>
#include <type_traits>

typedef unsigned short u16;
typedef unsigned int u32;
typedef unsigned long long u64;
typedef __attribute__((ext_vector_type(8))) __bf16 bf16x8;
typedef __attribute__((ext_vector_type(2))) __bf16 bf16x2;
typedef __attribute__((ext_vector_type(4))) short s16x4;
typedef __attribute__((ext_vector_type(16))) float f32x16;
typedef __attribute__((ext_vector_type(4))) float f32x4;
typedef __attribute__((ext_vector_type(4))) u32 u32x4;
typedef __attribute__((ext_vector_type(2))) u32 u32x2;
#define LAS __attribute__((address_space(3)))
#define GAS __attribute__((address_space(1)))
typedef LAS const char* lds_cptr;
typedef LAS char* lds_ptr;

constexpr int S_ = 2048, D_ = 512, H_ = 32, F_ = 1792;
constexpr float LN_EPS = 1e-5f;
constexpr float QSCALE = 0.25f * 1.4426950408889634f;

constexpr size_t WS_Q = 0;
constexpr size_t WS_K = WS_Q + (2u << 20);
constexpr size_t WS_V = WS_K + (2u << 20);
constexpr size_t WS_O = WS_V + (2u << 20);
constexpr size_t WS_H = WS_O + (2u << 20);
constexpr size_t WS_ST = WS_H + (2u << 20);
constexpr size_t WS_GR = WS_ST + 3 * 2048 * 4;
constexpr size_t WS_GA = WS_ST + (1u << 20);
constexpr size_t WS_GD = WS_GA + (4u << 20);

#define MFMA32(a, b, c) __builtin_amdgcn_mfma_f32_32x32x16_bf16((a), (b), (c), 0, 0, 0)

__device__ __forceinline__ float bf2f(u32 lo16) { return __uint_as_float(lo16 << 16); }
__device__ __forceinline__ float bfhi2f(u32 w) { return __uint_as_float(w & 0xffff0000u); }
__device__ __forceinline__ float bflo2f(u32 w) { return __uint_as_float(w << 16); }
__device__ __forceinline__ u32 pk_bf16(float lo, float hi) {
    bf16x2 v; v[0] = (__bf16)lo; v[1] = (__bf16)hi; return __builtin_bit_cast(u32, v);
}
__device__ __forceinline__ float rbf(float x) { return (float)(__bf16)x; }

__device__ __forceinline__ void glds16(u32 voff, const void* sbase, u32 lds_dst) {
    u32 keep;
    asm volatile("s_mov_b32 %0, m0\n\ts_mov_b32 m0, %3\n\ts_nop 2\n\tglobal_load_lds_dwordx4 %1, %2\n\ts_mov_b32 m0, %0"
                 : "=&s"(keep) : "v"(voff), "s"(sbase), "s"(lds_dst) : "memory");
}
__device__ __forceinline__ void touch4(const void* p, u32 lds_dst) {
    u32 keep;
    asm volatile("s_mov_b32 %0, m0\n\ts_mov_b32 m0, %2\n\ts_nop 0\n\tglobal_load_lds_dword %1, off\n\ts_mov_b32 m0, %0"
                 : "=&s"(keep) : "v"(p), "s"(lds_dst) : "memory");
}
template <bool WT> __device__ __forceinline__ void st16(void* p, const u32x4& v) {
    if constexpr (WT) asm volatile("global_store_dwordx4 %0, %1, off sc1\n\ts_nop 1" :: "v"(p), "v"(v) : "memory");
    else *(u32x4*)p = v;
}
#define WT_QKV true
#define WT_REL true
#define WT_O true
#define WT_H true
#define WT_OUT true
#define LDS_BAR() asm volatile("s_waitcnt lgkmcnt(0)\n\ts_barrier" ::: "memory")
#define WAIT_BAR(N) asm volatile("s_waitcnt vmcnt(" #N ") lgkmcnt(0)\n\ts_barrier" ::: "memory")

__device__ __forceinline__ bf16x8 lds_frag(lds_cptr p) { return *(LAS const bf16x8*)p; }
__device__ __forceinline__ s16x4 lds_tr(lds_cptr p) {
    return __builtin_amdgcn_ds_read_tr16_b64_v4i16((LAS s16x4*)p);
}
__device__ __forceinline__ bf16x8 join_tr(s16x4 a, s16x4 b) {
    typedef __attribute__((ext_vector_type(8))) short s16x8;
    s16x8 r = {a[0], a[1], a[2], a[3], b[0], b[1], b[2], b[3]};
    return __builtin_bit_cast(bf16x8, r);
}
__device__ __forceinline__ float half_sum(float v) {
    auto r = __builtin_amdgcn_permlane32_swap(__float_as_uint(v), __float_as_uint(v), false, false);
    return __uint_as_float(r[0]) + __uint_as_float(r[1]);
}
__device__ __forceinline__ float half_max(float v) {
    auto r = __builtin_amdgcn_permlane32_swap(__float_as_uint(v), __float_as_uint(v), false, false);
    return fmaxf(__uint_as_float(r[0]), __uint_as_float(r[1]));
}
template <int CTRL> __device__ __forceinline__ float dpp_f(float v) {
    return __uint_as_float(__builtin_amdgcn_update_dpp(0, __float_as_uint(v), CTRL, 0xf, 0xf, true));
}
template <int CTRL> __device__ __forceinline__ u32 dpp_u(u32 v) {
    return __builtin_amdgcn_update_dpp(0, v, CTRL, 0xf, 0xf, true);
}
__device__ __forceinline__ float row16_sum(float v) {
    v += dpp_f<0xB1>(v);
    v += dpp_f<0x4E>(v);
    v += dpp_f<0x141>(v);
    v += dpp_f<0x140>(v);
    return v;
}
__device__ __forceinline__ float wave_sum(float v) {
    v = row16_sum(v);
    auto r = __builtin_amdgcn_permlane16_swap(__float_as_uint(v), __float_as_uint(v), false, false);
    v = __uint_as_float(r[0]) + __uint_as_float(r[1]);
    return half_sum(v);
}
__device__ __forceinline__ u32 wave_umax(u32 v) {
    v = max(v, dpp_u<0xB1>(v));
    v = max(v, dpp_u<0x4E>(v));
    v = max(v, dpp_u<0x141>(v));
    v = max(v, dpp_u<0x140>(v));
    auto r = __builtin_amdgcn_permlane16_swap(v, v, false, false);
    v = max((u32)r[0], (u32)r[1]);
    auto q = __builtin_amdgcn_permlane32_swap(v, v, false, false);
    return max((u32)q[0], (u32)q[1]);
}
__device__ __forceinline__ float dot2(u32 a, u32 b, float c) {
    return __builtin_amdgcn_fdot2_f32_bf16(__builtin_bit_cast(bf16x2, a), __builtin_bit_cast(bf16x2, b), c, false);
}

#define CODE_ADDR(SYM, lo, hi) asm volatile("s_getpc_b64 s[20:21]\n\ts_add_u32 s20, s20, " #SYM "@rel32@lo+4\n\ts_addc_u32 s21, s21, " #SYM "@rel32@hi+12\n\t" \
                                            "s_mov_b32 %0, s20\n\ts_mov_b32 %1, s21" : "=s"(lo), "=s"(hi) :: "s20", "s21", "scc")
#define CODE_PREFETCH(FROM, TO, part, sink)     \
    do { u32 al_, ah_, bl_, bh_; CODE_ADDR(FROM, al_, ah_); CODE_ADDR(TO, bl_, bh_); \
    const u64 a_ = ((u64)ah_ << 32) | al_, b_ = ((u64)bh_ << 32) | bl_; \
    u64 p_ = a_ + (u64)(part) * 8192 + (u64)lane * 128; if (!(b_ > a_ && b_ - a_ < 65536 && p_ < b_)) p_ = a_; \
    touch4((const void*)p_, sink); } while (0)

__device__ __forceinline__ u32x4 norm8(const u32x4& v, float a, float b) {
    float f[8];
#pragma unroll
    for (int i = 0; i < 4; ++i) { f[2 * i] = bflo2f(v[i]); f[2 * i + 1] = bfhi2f(v[i]); }
    asm volatile("" : "+v"(f[0]), "+v"(f[1]), "+v"(f[2]), "+v"(f[3]), "+v"(f[4]), "+v"(f[5]), "+v"(f[6]), "+v"(f[7]));
#pragma unroll
    for (int i = 0; i < 8; ++i) f[i] = fmaf(f[i], a, b);
    asm volatile("" : "+v"(f[0]), "+v"(f[1]), "+v"(f[2]), "+v"(f[3]), "+v"(f[4]), "+v"(f[5]), "+v"(f[6]), "+v"(f[7]));
    return u32x4{pk_bf16(f[0], f[1]), pk_bf16(f[2], f[3]), pk_bf16(f[4], f[5]), pk_bf16(f[6], f[7])};
}

constexpr int K1_WOFF = 65536, K1_WBUF = 24576, K1_LDS = K1_WOFF + 4 * K1_WBUF;
extern "C" __global__ __launch_bounds__(512, 2) void k1_qkv(const u16* __restrict__ x, const u16* __restrict__ wqkv,
                                                 char* __restrict__ ws) {
    extern __shared__ __attribute__((aligned(16))) char lds[];
    const int tid = threadIdx.x, lane = tid & 63;
    const int w8 = __builtin_amdgcn_readfirstlane(tid >> 6);
    const int w = w8 & 3;
    const int xcd = blockIdx.x & 7, jx = blockIdx.x >> 3;
    const int rb = 8 * (xcd >> 1) + (jx & 7), cb = 4 * (xcd & 1) + (jx >> 3);
    const u32 lds0 = (u32)(uintptr_t)lds;
    const int r8 = lane >> 3, c8 = lane & 7;

    {
        const u32 xoff = (u32)(8 * w8 + r8) * 1024 + (u32)((c8 ^ ((4 * w8 + (r8 >> 1)) & 7)) * 16);
        const char* xb = (const char*)x + (size_t)rb * 64 * 1024;
#pragma unroll
        for (int c = 0; c < 8; ++c) glds16(xoff, xb + c * 128, lds0 + (u32)c * 8192 + (u32)w8 * 1024);
    }
    u32 touch;
    {
        const char* tp = (const char*)wqkv + (size_t)(16 * jx + (lane >> 2)) * 3072 + (w8 % 3) * 1024 + (4 * (xcd & 1) + (lane & 3)) * 128;
        if (w8 < 5) tp = (const char*)wqkv + (size_t)(16 * jx) * 3072;
        asm volatile("global_load_dword %0, %1, off" : "=v"(touch) : "v"(tp) : "memory");
    }
    const u32 woff = (u32)(16 * w + r8) * 3072 + (u32)((c8 ^ (((lane >> 4) & 1) << 2)) * 16);
    const char* wb = (const char*)wqkv + cb * 128;
    auto issue_w = [&](int c) {
        const u32 dst = lds0 + K1_WOFF + (u32)(c & 3) * K1_WBUF + (u32)w * 2048;
#pragma unroll
        for (int s = 0; s < 3; ++s) {
            glds16(woff, wb + (size_t)c * 64 * 3072 + s * 1024, dst + s * 8192);
            glds16(woff, wb + (size_t)c * 64 * 3072 + s * 1024 + 8 * 3072, dst + s * 8192 + 1024);
        }
    };
    {
        LAS u32x4* xp = (LAS u32x4*)((lds_ptr)lds + w8 * 1024 + lane * 16);
        u32x4 xv[8];
        float s1[2] = {}, s2[2] = {};
#pragma unroll
        for (int c = 0; c < 8; ++c) {
            if (c == 0) asm volatile("s_waitcnt vmcnt(7)" ::: "memory");
            else if (c == 2) asm volatile("s_waitcnt vmcnt(5)" ::: "memory");
            else if (c == 4) {
                asm volatile("s_waitcnt vmcnt(3)" ::: "memory");
                issue_w(w8 >= 4 ? 0 : 1);
            }
            else if (c == 6) asm volatile("s_waitcnt vmcnt(6)" :: "v"(touch) : "memory");
            xv[c] = xp[c * 512];
#pragma unroll
            for (int i = 0; i < 4; ++i) { s1[i & 1] = dot2(xv[c][i], 0x3f803f80u, s1[i & 1]); s2[i & 1] = dot2(xv[c][i], xv[c][i], s2[i & 1]); }
        }
        float t1 = s1[0] + s1[1], t2 = s2[0] + s2[1];
        t1 += dpp_f<0xB1>(t1); t2 += dpp_f<0xB1>(t2);
        t1 += dpp_f<0x4E>(t1); t2 += dpp_f<0x4E>(t2);
        t1 += dpp_f<0x141>(t1); t2 += dpp_f<0x141>(t2);
        const float mean = t1 * (1.f / 512.f);
        const float var = fmaxf(t2 * (1.f / 512.f) - mean * mean, 0.f);
        const float mb = rbf(mean);
        const float den = rbf(__builtin_amdgcn_sqrtf(rbf(rbf(var) + LN_EPS)));
        const float rden = __builtin_amdgcn_rcpf(den), noff = -mb * rden;
#pragma unroll
        for (int c = 0; c < 8; ++c) xp[c * 512] = norm8(xv[c], rden, noff);
    }
    if (w8 >= 4) {
        WAIT_BAR(0);
        asm volatile("s_barrier" ::: "memory");
        asm volatile("s_barrier" ::: "memory");
#pragma unroll 1
        for (int c = 3; c < 7; ++c) { issue_w(c + 1); WAIT_BAR(6); }
        WAIT_BAR(0);
        if (blockIdx.x < 8) {
            const u32 i = blockIdx.x * 256 + (tid - 256);
            const u32 v = i < 1536 ? 0u : 0xffffffffu;
            if (i < 1536 + 448) *(u32x4*)(ws + WS_ST + (size_t)i * 16) = u32x4{v, v, v, v};
        }
        return;
    }

    issue_w(2); issue_w(3);
    const int rt = w & 1, ch = w >> 1;
    const int r32 = lane & 31, h = lane >> 5;
    const int xrow = 32 * rt + r32;
    const int xt = h ^ ((xrow >> 1) & 7);
    u32 xa[4], wa[2];
#pragma unroll
    for (int ks = 0; ks < 4; ++ks) xa[ks] = lds0 + xrow * 128 + ((xt ^ (2 * ks)) << 4);
    const int q4 = (lane & 15) >> 2, p4 = lane & 3, cg = (lane >> 4) & 1;
    const int wchunk = (4 * ch + 2 * cg + (p4 >> 1)) ^ ((q4 >> 1) << 2);
    wa[0] = lds0 + K1_WOFF + (8 * h + q4) * 128 + wchunk * 16 + 8 * (p4 & 1); wa[1] = wa[0] + 2 * K1_WBUF;
#pragma unroll
    for (int ks = 0; ks < 4; ++ks) asm volatile("" : "+v"(xa[ks]));
    asm volatile("" : "+v"(wa[0]), "+v"(wa[1]));

    f32x16 aq = {}, ak = {}, av = {};
    struct Frags { bf16x8 x, q, k, v; };
    auto load = [&](int c, int ks) {
        Frags f;
        const int sl = c & 3;
        const lds_cptr xq = (lds_cptr)(uintptr_t)xa[ks] + c * 8192;
        const lds_cptr wp = (lds_cptr)(uintptr_t)wa[sl >> 1] + (sl & 1) * K1_WBUF + ks * 2048;
        f.x = lds_frag(xq);
        f.q = join_tr(lds_tr(wp), lds_tr(wp + 512));
        f.k = join_tr(lds_tr(wp + 8192), lds_tr(wp + 8192 + 512));
        f.v = join_tr(lds_tr(wp + 16384), lds_tr(wp + 16384 + 512));
        return f;
    };
    asm volatile("s_waitcnt lgkmcnt(0)\n\ts_barrier" ::: "memory");
    Frags fr[3];
    fr[0] = load(0, 0); fr[1] = load(0, 1);
#pragma unroll
    for (int st = 0; st < 32; ++st) {
        const int c = st >> 2, ks = st & 3;
        const Frags& cur = fr[st % 3];
        if (st + 2 < 32) fr[(st + 2) % 3] = load((st + 2) >> 2, (st + 2) & 3);
        aq = MFMA32(cur.q, cur.x, aq);
        ak = MFMA32(cur.k, cur.x, ak);
        av = MFMA32(cur.x, cur.v, av);
        __builtin_amdgcn_sched_group_barrier(0x8, 1, 0); __builtin_amdgcn_sched_group_barrier(0x100, 3, 0);
        __builtin_amdgcn_sched_group_barrier(0x8, 1, 0); __builtin_amdgcn_sched_group_barrier(0x100, 2, 0);
        __builtin_amdgcn_sched_group_barrier(0x8, 1, 0); __builtin_amdgcn_sched_group_barrier(0x100, 2, 0);
        __builtin_amdgcn_sched_barrier(0);
        if (ks == 1 && c == 0) asm volatile("s_waitcnt vmcnt(12)\n\ts_barrier" ::: "memory");
        else if (ks == 1 && c == 1) asm volatile("s_waitcnt vmcnt(6)\n\ts_barrier" ::: "memory");
        else if (ks == 1 && c == 2) asm volatile("s_waitcnt vmcnt(0)\n\ts_barrier" ::: "memory");
        else if (ks == 1 && c < 7) asm volatile("s_barrier" ::: "memory");
        if (st == 9 && jx == 0) CODE_PREFETCH(k2_attn, k9_end, w, lds0 + (u32)w * 1024);
    }
    const int head0 = cb * 4 + ch * 2, tile = rb * 2 + rt;
    {
        const size_t o0 = ((size_t)(head0 * 64 + tile) * 64 + lane) * 16, o1 = o0 + 64 * 1024;
        st16<WT_QKV>(ws + WS_K + o0, u32x4{pk_bf16(ak[0], ak[1]), pk_bf16(ak[2], ak[3]), pk_bf16(ak[4], ak[5]), pk_bf16(ak[6], ak[7])});
        st16<WT_QKV>(ws + WS_K + o1, u32x4{pk_bf16(ak[8], ak[9]), pk_bf16(ak[10], ak[11]), pk_bf16(ak[12], ak[13]), pk_bf16(ak[14], ak[15])});
#pragma unroll
        for (int i = 0; i < 16; ++i) aq[i] *= QSCALE;
        st16<WT_QKV>(ws + WS_Q + o0, u32x4{pk_bf16(aq[0], aq[1]), pk_bf16(aq[2], aq[3]), pk_bf16(aq[4], aq[5]), pk_bf16(aq[6], aq[7])});
        st16<WT_QKV>(ws + WS_Q + o1, u32x4{pk_bf16(aq[8], aq[9]), pk_bf16(aq[10], aq[11]), pk_bf16(aq[12], aq[13]), pk_bf16(aq[14], aq[15])});
    }
    {
        const size_t o = (size_t)((head0 + (r32 >> 4)) * 64 + tile) * 1024 + h * 256 + (r32 & 15) * 16;
        st16<WT_QKV>(ws + WS_V + o, u32x4{pk_bf16(av[0], av[1]), pk_bf16(av[2], av[3]), pk_bf16(av[4], av[5]), pk_bf16(av[6], av[7])});
        st16<WT_QKV>(ws + WS_V + o + 512, u32x4{pk_bf16(av[8], av[9]), pk_bf16(av[10], av[11]), pk_bf16(av[12], av[13]), pk_bf16(av[14], av[15])});
    }
}

constexpr int K2_VSTRIDE = 1056;
constexpr int K2_VOFF = 65536 + 2048;
constexpr int K2_SCR = K2_VOFF + 65 * K2_VSTRIDE + 32;
constexpr int K2_SINK = K2_SCR + 3 * 8192;
constexpr int K2_LDS = K2_SINK + 256;

__device__ __forceinline__ float max16(const f32x16& s) {
    float a = fmaxf(fmaxf(s[0], s[1]), s[2]), b = fmaxf(fmaxf(s[3], s[4]), s[5]);
    a = fmaxf(fmaxf(a, s[6]), s[7]); b = fmaxf(fmaxf(b, s[8]), s[9]);
    a = fmaxf(fmaxf(a, s[10]), s[11]); b = fmaxf(fmaxf(b, s[12]), s[13]);
    a = fmaxf(fmaxf(a, s[14]), s[15]);
    return fmaxf(a, b);
}
__device__ __forceinline__ void exp_pack(const f32x16& s, u32x4& p0, u32x4& p1) {
#pragma unroll
    for (int i = 0; i < 4; ++i) {
        p0[i] = pk_bf16(__builtin_amdgcn_exp2f(s[2 * i]), __builtin_amdgcn_exp2f(s[2 * i + 1]));
        p1[i] = pk_bf16(__builtin_amdgcn_exp2f(s[8 + 2 * i]), __builtin_amdgcn_exp2f(s[8 + 2 * i + 1]));
    }
}

#define K2_LOOP_BODY \
    "s_waitcnt lgkmcnt(2)\n\t" \
    "v_mfma_f32_32x32x16_bf16 v[32:47], v[68:71], v[64:67], v[48:63]\n\t" \
    "ds_read_b128 v[68:71], v88 offset:2048\n\t" \
    "v_exp_f32 v16, v16\n\t" \
    "v_exp_f32 v17, v17\n\t" \
    "v_exp_f32 v18, v18\n\t" \
    "v_exp_f32 v19, v19\n\t" \
    "s_waitcnt lgkmcnt(2)\n\t" \
    "v_mfma_f32_32x32x16_bf16 v[0:15], v[76:79], v[84:87], v[0:15]\n\t" \
    "ds_read_b128 v[76:79], v90 offset:0\n\t" \
    "v_cvt_pk_bf16_f32 v80, v16, v17\n\t" \
    "v_exp_f32 v20, v20\n\t" \
    "v_exp_f32 v21, v21\n\t" \
    "v_cvt_pk_bf16_f32 v81, v18, v19\n\t" \
    "v_exp_f32 v22, v22\n\t" \
    "v_exp_f32 v23, v23\n\t" \
    "v_cvt_pk_bf16_f32 v82, v20, v21\n\t" \
    "v_exp_f32 v24, v24\n\t" \
    "v_exp_f32 v25, v25\n\t" \
    "v_cvt_pk_bf16_f32 v83, v22, v23\n\t" \
    "v_exp_f32 v26, v26\n\t" \
    "v_exp_f32 v27, v27\n\t" \
    "s_waitcnt lgkmcnt(2)\n\t" \
    "v_mfma_f32_32x32x16_bf16 v[0:15], v[72:75], v[80:83], v[0:15]\n\t" \
    "ds_read_b128 v[72:75], v89 offset:1056\n\t" \
    "v_cvt_pk_bf16_f32 v84, v24, v25\n\t" \
    "v_exp_f32 v28, v28\n\t" \
    "v_exp_f32 v29, v29\n\t" \
    "v_cvt_pk_bf16_f32 v85, v26, v27\n\t" \
    "v_exp_f32 v30, v30\n\t" \
    "v_exp_f32 v31, v31\n\t" \
    "v_cvt_pk_bf16_f32 v86, v28, v29\n\t" \
    "v_cvt_pk_bf16_f32 v87, v30, v31\n\t" \
    "s_waitcnt lgkmcnt(2)\n\t" \
    "v_mfma_f32_32x32x16_bf16 v[16:31], v[68:71], v[64:67], v[48:63]\n\t" \
    "ds_read_b128 v[68:71], v88 offset:3072\n\t" \
    "v_exp_f32 v32, v32\n\t" \
    "v_exp_f32 v33, v33\n\t" \
    "v_exp_f32 v34, v34\n\t" \
    "v_exp_f32 v35, v35\n\t" \
    "s_waitcnt lgkmcnt(2)\n\t" \
    "v_mfma_f32_32x32x16_bf16 v[0:15], v[76:79], v[84:87], v[0:15]\n\t" \
    "ds_read_b128 v[76:79], v90 offset:1056\n\t" \
    "v_cvt_pk_bf16_f32 v80, v32, v33\n\t" \
    "v_exp_f32 v36, v36\n\t" \
    "v_exp_f32 v37, v37\n\t" \
    "v_cvt_pk_bf16_f32 v81, v34, v35\n\t" \
    "v_exp_f32 v38, v38\n\t" \
    "v_exp_f32 v39, v39\n\t" \
    "v_cvt_pk_bf16_f32 v82, v36, v37\n\t" \
    "v_exp_f32 v40, v40\n\t" \
    "v_exp_f32 v41, v41\n\t" \
    "v_cvt_pk_bf16_f32 v83, v38, v39\n\t" \
    "v_exp_f32 v42, v42\n\t" \
    "v_exp_f32 v43, v43\n\t" \
    "s_waitcnt lgkmcnt(2)\n\t" \
    "v_mfma_f32_32x32x16_bf16 v[0:15], v[72:75], v[80:83], v[0:15]\n\t" \
    "ds_read_b128 v[72:75], v89 offset:2112\n\t" \
    "v_cvt_pk_bf16_f32 v84, v40, v41\n\t" \
    "v_exp_f32 v44, v44\n\t" \
    "v_exp_f32 v45, v45\n\t" \
    "v_cvt_pk_bf16_f32 v85, v42, v43\n\t" \
    "v_exp_f32 v46, v46\n\t" \
    "v_exp_f32 v47, v47\n\t" \
    "v_cvt_pk_bf16_f32 v86, v44, v45\n\t" \
    "v_cvt_pk_bf16_f32 v87, v46, v47\n\t" \
    "s_waitcnt lgkmcnt(2)\n\t" \
    "v_mfma_f32_32x32x16_bf16 v[32:47], v[68:71], v[64:67], v[48:63]\n\t" \
    "ds_read_b128 v[68:71], v88 offset:4096\n\t" \
    "v_exp_f32 v16, v16\n\t" \
    "v_exp_f32 v17, v17\n\t" \
    "v_exp_f32 v18, v18\n\t" \
    "v_exp_f32 v19, v19\n\t" \
    "s_waitcnt lgkmcnt(2)\n\t" \
    "v_mfma_f32_32x32x16_bf16 v[0:15], v[76:79], v[84:87], v[0:15]\n\t" \
    "ds_read_b128 v[76:79], v90 offset:2112\n\t" \
    "v_cvt_pk_bf16_f32 v80, v16, v17\n\t" \
    "v_exp_f32 v20, v20\n\t" \
    "v_exp_f32 v21, v21\n\t" \
    "v_cvt_pk_bf16_f32 v81, v18, v19\n\t" \
    "v_exp_f32 v22, v22\n\t" \
    "v_exp_f32 v23, v23\n\t" \
    "v_cvt_pk_bf16_f32 v82, v20, v21\n\t" \
    "v_exp_f32 v24, v24\n\t" \
    "v_exp_f32 v25, v25\n\t" \
    "v_cvt_pk_bf16_f32 v83, v22, v23\n\t" \
    "v_exp_f32 v26, v26\n\t" \
    "v_exp_f32 v27, v27\n\t" \
    "s_waitcnt lgkmcnt(2)\n\t" \
    "v_mfma_f32_32x32x16_bf16 v[0:15], v[72:75], v[80:83], v[0:15]\n\t" \
    "ds_read_b128 v[72:75], v89 offset:3168\n\t" \
    "v_cvt_pk_bf16_f32 v84, v24, v25\n\t" \
    "v_exp_f32 v28, v28\n\t" \
    "v_exp_f32 v29, v29\n\t" \
    "v_cvt_pk_bf16_f32 v85, v26, v27\n\t" \
    "v_exp_f32 v30, v30\n\t" \
    "v_exp_f32 v31, v31\n\t" \
    "v_cvt_pk_bf16_f32 v86, v28, v29\n\t" \
    "v_cvt_pk_bf16_f32 v87, v30, v31\n\t" \
    "s_waitcnt lgkmcnt(2)\n\t" \
    "v_mfma_f32_32x32x16_bf16 v[16:31], v[68:71], v[64:67], v[48:63]\n\t" \
    "ds_read_b128 v[68:71], v88 offset:5120\n\t" \
    "v_exp_f32 v32, v32\n\t" \
    "v_exp_f32 v33, v33\n\t" \
    "v_exp_f32 v34, v34\n\t" \
    "v_exp_f32 v35, v35\n\t" \
    "s_waitcnt lgkmcnt(2)\n\t" \
    "v_mfma_f32_32x32x16_bf16 v[0:15], v[76:79], v[84:87], v[0:15]\n\t" \
    "ds_read_b128 v[76:79], v90 offset:3168\n\t" \
    "v_cvt_pk_bf16_f32 v80, v32, v33\n\t" \
    "v_exp_f32 v36, v36\n\t" \
    "v_exp_f32 v37, v37\n\t" \
    "v_cvt_pk_bf16_f32 v81, v34, v35\n\t" \
    "v_exp_f32 v38, v38\n\t" \
    "v_exp_f32 v39, v39\n\t" \
    "v_cvt_pk_bf16_f32 v82, v36, v37\n\t" \
    "v_exp_f32 v40, v40\n\t" \
    "v_exp_f32 v41, v41\n\t" \
    "v_cvt_pk_bf16_f32 v83, v38, v39\n\t" \
    "v_exp_f32 v42, v42\n\t" \
    "v_exp_f32 v43, v43\n\t" \
    "s_waitcnt lgkmcnt(2)\n\t" \
    "v_mfma_f32_32x32x16_bf16 v[0:15], v[72:75], v[80:83], v[0:15]\n\t" \
    "ds_read_b128 v[72:75], v89 offset:4224\n\t" \
    "v_cvt_pk_bf16_f32 v84, v40, v41\n\t" \
    "v_exp_f32 v44, v44\n\t" \
    "v_exp_f32 v45, v45\n\t" \
    "v_cvt_pk_bf16_f32 v85, v42, v43\n\t" \
    "v_exp_f32 v46, v46\n\t" \
    "v_exp_f32 v47, v47\n\t" \
    "v_cvt_pk_bf16_f32 v86, v44, v45\n\t" \
    "v_cvt_pk_bf16_f32 v87, v46, v47\n\t" \
    "s_waitcnt lgkmcnt(2)\n\t" \
    "v_mfma_f32_32x32x16_bf16 v[32:47], v[68:71], v[64:67], v[48:63]\n\t" \
    "ds_read_b128 v[68:71], v88 offset:6144\n\t" \
    "v_exp_f32 v16, v16\n\t" \
    "v_exp_f32 v17, v17\n\t" \
    "v_exp_f32 v18, v18\n\t" \
    "v_exp_f32 v19, v19\n\t" \
    "s_waitcnt lgkmcnt(2)\n\t" \
    "v_mfma_f32_32x32x16_bf16 v[0:15], v[76:79], v[84:87], v[0:15]\n\t" \
    "ds_read_b128 v[76:79], v90 offset:4224\n\t" \
    "v_cvt_pk_bf16_f32 v80, v16, v17\n\t" \
    "v_exp_f32 v20, v20\n\t" \
    "v_exp_f32 v21, v21\n\t" \
    "v_cvt_pk_bf16_f32 v81, v18, v19\n\t" \
    "v_exp_f32 v22, v22\n\t" \
    "v_exp_f32 v23, v23\n\t" \
    "v_cvt_pk_bf16_f32 v82, v20, v21\n\t" \
    "v_exp_f32 v24, v24\n\t" \
    "v_exp_f32 v25, v25\n\t" \
    "v_cvt_pk_bf16_f32 v83, v22, v23\n\t" \
    "v_exp_f32 v26, v26\n\t" \
    "v_exp_f32 v27, v27\n\t" \
    "s_waitcnt lgkmcnt(2)\n\t" \
    "v_mfma_f32_32x32x16_bf16 v[0:15], v[72:75], v[80:83], v[0:15]\n\t" \
    "ds_read_b128 v[72:75], v89 offset:5280\n\t" \
    "v_cvt_pk_bf16_f32 v84, v24, v25\n\t" \
    "v_exp_f32 v28, v28\n\t" \
    "v_exp_f32 v29, v29\n\t" \
    "v_cvt_pk_bf16_f32 v85, v26, v27\n\t" \
    "v_exp_f32 v30, v30\n\t" \
    "v_exp_f32 v31, v31\n\t" \
    "v_cvt_pk_bf16_f32 v86, v28, v29\n\t" \
    "v_cvt_pk_bf16_f32 v87, v30, v31\n\t" \
    "s_waitcnt lgkmcnt(2)\n\t" \
    "v_mfma_f32_32x32x16_bf16 v[16:31], v[68:71], v[64:67], v[48:63]\n\t" \
    "ds_read_b128 v[68:71], v88 offset:7168\n\t" \
    "v_exp_f32 v32, v32\n\t" \
    "v_exp_f32 v33, v33\n\t" \
    "v_exp_f32 v34, v34\n\t" \
    "v_exp_f32 v35, v35\n\t" \
    "s_waitcnt lgkmcnt(2)\n\t" \
    "v_mfma_f32_32x32x16_bf16 v[0:15], v[76:79], v[84:87], v[0:15]\n\t" \
    "ds_read_b128 v[76:79], v90 offset:5280\n\t" \
    "v_cvt_pk_bf16_f32 v80, v32, v33\n\t" \
    "v_exp_f32 v36, v36\n\t" \
    "v_exp_f32 v37, v37\n\t" \
    "v_cvt_pk_bf16_f32 v81, v34, v35\n\t" \
    "v_exp_f32 v38, v38\n\t" \
    "v_exp_f32 v39, v39\n\t" \
    "v_cvt_pk_bf16_f32 v82, v36, v37\n\t" \
    "v_exp_f32 v40, v40\n\t" \
    "v_exp_f32 v41, v41\n\t" \
    "v_cvt_pk_bf16_f32 v83, v38, v39\n\t" \
    "v_exp_f32 v42, v42\n\t" \
    "v_exp_f32 v43, v43\n\t" \
    "s_waitcnt lgkmcnt(2)\n\t" \
    "v_mfma_f32_32x32x16_bf16 v[0:15], v[72:75], v[80:83], v[0:15]\n\t" \
    "ds_read_b128 v[72:75], v89 offset:6336\n\t" \
    "v_cvt_pk_bf16_f32 v84, v40, v41\n\t" \
    "v_exp_f32 v44, v44\n\t" \
    "v_exp_f32 v45, v45\n\t" \
    "v_cvt_pk_bf16_f32 v85, v42, v43\n\t" \
    "v_exp_f32 v46, v46\n\t" \
    "v_exp_f32 v47, v47\n\t" \
    "v_cvt_pk_bf16_f32 v86, v44, v45\n\t" \
    "v_cvt_pk_bf16_f32 v87, v46, v47\n\t" \
    "s_waitcnt lgkmcnt(2)\n\t" \
    "v_mfma_f32_32x32x16_bf16 v[32:47], v[68:71], v[64:67], v[48:63]\n\t" \
    "ds_read_b128 v[68:71], v88 offset:8192\n\t" \
    "v_exp_f32 v16, v16\n\t" \
    "v_exp_f32 v17, v17\n\t" \
    "v_exp_f32 v18, v18\n\t" \
    "v_exp_f32 v19, v19\n\t" \
    "s_waitcnt lgkmcnt(2)\n\t" \
    "v_mfma_f32_32x32x16_bf16 v[0:15], v[76:79], v[84:87], v[0:15]\n\t" \
    "ds_read_b128 v[76:79], v90 offset:6336\n\t" \
    "v_cvt_pk_bf16_f32 v80, v16, v17\n\t" \
    "v_exp_f32 v20, v20\n\t" \
    "v_exp_f32 v21, v21\n\t" \
    "v_cvt_pk_bf16_f32 v81, v18, v19\n\t" \
    "v_exp_f32 v22, v22\n\t" \
    "v_exp_f32 v23, v23\n\t" \
    "v_cvt_pk_bf16_f32 v82, v20, v21\n\t" \
    "v_exp_f32 v24, v24\n\t" \
    "v_exp_f32 v25, v25\n\t" \
    "v_cvt_pk_bf16_f32 v83, v22, v23\n\t" \
    "v_exp_f32 v26, v26\n\t" \
    "v_exp_f32 v27, v27\n\t" \
    "s_waitcnt lgkmcnt(2)\n\t" \
    "v_mfma_f32_32x32x16_bf16 v[0:15], v[72:75], v[80:83], v[0:15]\n\t" \
    "ds_read_b128 v[72:75], v89 offset:7392\n\t" \
    "v_cvt_pk_bf16_f32 v84, v24, v25\n\t" \
    "v_exp_f32 v28, v28\n\t" \
    "v_exp_f32 v29, v29\n\t" \
    "v_cvt_pk_bf16_f32 v85, v26, v27\n\t" \
    "v_exp_f32 v30, v30\n\t" \
    "v_exp_f32 v31, v31\n\t" \
    "v_cvt_pk_bf16_f32 v86, v28, v29\n\t" \
    "v_cvt_pk_bf16_f32 v87, v30, v31\n\t" \
    "s_waitcnt lgkmcnt(2)\n\t" \
    "v_mfma_f32_32x32x16_bf16 v[16:31], v[68:71], v[64:67], v[48:63]\n\t" \
    "ds_read_b128 v[68:71], v88 offset:9216\n\t" \
    "v_exp_f32 v32, v32\n\t" \
    "v_exp_f32 v33, v33\n\t" \
    "v_exp_f32 v34, v34\n\t" \
    "v_exp_f32 v35, v35\n\t" \
    "s_waitcnt lgkmcnt(2)\n\t" \
    "v_mfma_f32_32x32x16_bf16 v[0:15], v[76:79], v[84:87], v[0:15]\n\t" \
    "ds_read_b128 v[76:79], v90 offset:7392\n\t" \
    "v_cvt_pk_bf16_f32 v80, v32, v33\n\t" \
    "v_exp_f32 v36, v36\n\t" \
    "v_exp_f32 v37, v37\n\t" \
    "v_cvt_pk_bf16_f32 v81, v34, v35\n\t" \
    "v_exp_f32 v38, v38\n\t" \
    "v_exp_f32 v39, v39\n\t" \
    "v_cvt_pk_bf16_f32 v82, v36, v37\n\t" \
    "v_exp_f32 v40, v40\n\t" \
    "v_exp_f32 v41, v41\n\t" \
    "v_cvt_pk_bf16_f32 v83, v38, v39\n\t" \
    "v_exp_f32 v42, v42\n\t" \
    "v_exp_f32 v43, v43\n\t" \
    "s_waitcnt lgkmcnt(2)\n\t" \
    "v_mfma_f32_32x32x16_bf16 v[0:15], v[72:75], v[80:83], v[0:15]\n\t" \
    "ds_read_b128 v[72:75], v89 offset:8448\n\t" \
    "v_cvt_pk_bf16_f32 v84, v40, v41\n\t" \
    "v_exp_f32 v44, v44\n\t" \
    "v_exp_f32 v45, v45\n\t" \
    "v_cvt_pk_bf16_f32 v85, v42, v43\n\t" \
    "v_exp_f32 v46, v46\n\t" \
    "v_exp_f32 v47, v47\n\t" \
    "v_cvt_pk_bf16_f32 v86, v44, v45\n\t" \
    "v_cvt_pk_bf16_f32 v87, v46, v47\n\t" \
    "v_add_u32 v88, 8192, v88\n\t" \
    "v_add_u32 v89, 8448, v89\n\t" \
    "v_add_u32 v90, 8448, v90\n\t" \
    ""

#define K2_RUN(NIT) do { int nit_ = (NIT); \
    asm volatile("s_nop 1\n" "1:\n\t" K2_LOOP_BODY "s_sub_u32 %[n], %[n], 1\n\ts_cmp_lg_u32 %[n], 0\n\ts_cbranch_scc1 1b\n\ts_waitcnt lgkmcnt(0)" \
        : "+{v[0:15]}"(o), "+{v[16:31]}"(sa), "+{v[32:47]}"(sb), "+{v[68:71]}"(kf), "+{v[72:75]}"(v0f), "+{v[76:79]}"(v1f), \
          "=&{v[80:83]}"(p0), "+{v[84:87]}"(p1), "+{v88}"(ka), "+{v89}"(va0), "+{v90}"(va1), [n] "+s"(nit_) \
        : "{v[48:63]}"(negm), "{v[64:67]}"(qf) : "memory", "scc"); } while (0)

extern "C" __global__ __launch_bounds__(512, 2) void k2_attn(char* __restrict__ ws, const u16* __restrict__ x, const u16* __restrict__ wo,
                                                  const u16* __restrict__ wg, const u16* __restrict__ wu, const u16* __restrict__ wd) {
    extern __shared__ __attribute__((aligned(16))) char lds[];
    const int tid = threadIdx.x, lane = tid & 63;
    const int w = __builtin_amdgcn_readfirstlane(tid >> 6);
    const int b = blockIdx.x;
    const int head = 4 * (b & 7) + ((b >> 3) & 3), qt = (b >> 5) * 8 + w;
    const u32 lds0 = (u32)(uintptr_t)lds;
    const int r32 = lane & 31, h = lane >> 5;

    u32x4 qraw;
    {
        const char* qp = ws + WS_Q + ((size_t)(head * 64 + qt) * 64 + lane) * 16;
        asm volatile("global_load_dwordx4 %0, %1, off" : "=v"(qraw) : "v"(qp) : "memory");
    }
    const char* kb = ws + WS_K + (size_t)head * 65536;
    const char* vb = ws + WS_V + (size_t)head * 65536;
    const u32 loff = (u32)lane * 16;
    auto issue = [&](int c) {
        const int t = 8 * c + w;
        glds16(loff, kb + t * 1024, lds0 + (u32)t * 1024);
        glds16(loff, vb + t * 1024, lds0 + K2_VOFF + (u32)t * K2_VSTRIDE);
    };
    issue(0); issue(1); issue(2);

    const lds_cptr kp = (lds_cptr)lds + lane * 16;
    const int vpad = (r32 == 16 || r32 == 20) ? 1024 : 1040;
    const lds_cptr vp0 = (lds_cptr)lds + K2_VOFF + (r32 < 16 ? h * 256 + r32 * 16 : vpad);
    const lds_cptr vp1 = (lds_cptr)lds + K2_VOFF + (r32 < 16 ? 512 + h * 256 + r32 * 16 : vpad);

    asm volatile("s_waitcnt vmcnt(5)" : "+v"(qraw) :: "memory");
    const bf16x8 qf = __builtin_bit_cast(bf16x8, qraw);
    f32x16 negm;
    {
        f32x16 z = {};
        f32x16 s0 = MFMA32(lds_frag(kp + w * 1024), qf, z);
        const float m0 = half_max(max16(s0));
#pragma unroll
        for (int i = 0; i < 16; ++i) negm[i] = -m0;
    }
    if (tid < 65) {
        *(LAS u32x4*)((lds_ptr)lds + K2_VOFF + tid * K2_VSTRIDE + 1024) = u32x4{0x3f803f80u, 0x3f803f80u, 0x3f803f80u, 0x3f803f80u};
        *(LAS u32x4*)((lds_ptr)lds + K2_VOFF + tid * K2_VSTRIDE + 1040) = u32x4{0u, 0u, 0u, 0u};
    }
    WAIT_BAR(2);
    const bf16x8 k0 = lds_frag(kp);
    f32x16 o = {};
    f32x16 sa = MFMA32(k0, qf, negm), sb = {};
    bf16x8 kf = lds_frag(kp + 1024), v0f = lds_frag(vp0), v1f = lds_frag(vp1);
    u32x4 p0, p1 = {};
    u32 ka = (u32)(uintptr_t)kp, va0 = (u32)(uintptr_t)vp0, va1 = (u32)(uintptr_t)vp1;
    asm volatile("s_nop 7\n\ts_nop 7" : "+v"(sa) :: "memory");
    const int xcd = b & 7, slot = (b >> 3) * 3 + w;
    const bool dn_job = slot < 32;
    const int jG = dn_job ? xcd : xcd + 8 * ((slot - 32) >> 4);
    const bool job = w < 3 && (dn_job || jG < 28);
    const int jc = slot & 7, jq = dn_job ? (slot >> 3) : ((slot >> 3) & 1);
#pragma unroll
    for (int it = 0; it < 5; ++it) {
        K2_RUN(it < 2 ? 1 : 2);
        if (it == 0) { issue(3); issue(4); WAIT_BAR(4); }
        else if (it == 1) { issue(5); issue(6); issue(7); WAIT_BAR(6); }
        else if (it == 2) {
            WAIT_BAR(0);
            if (job) {
                const u32 pitch = dn_job ? 1024u : 3584u;
                const char* src = dn_job ? (const char*)wd + (size_t)(512 * jq + jc) * 1024 + jG * 128
                                         : (const char*)(jq ? wu : wg) + (size_t)jc * 3584 + jG * 128;
                const int lmax = (dn_job && jq == 3) ? 31 : 63;
#pragma unroll 1
                for (int pc = 0; pc < 8; ++pc) {
                    const int l = 8 * pc + (lane >> 3);
                    const u32 off = (u32)min(l, lmax) * 8u * pitch + (u32)(((lane & 7) ^ ((l >> 1) & 7)) * 16);
                    glds16(off, src, lds0 + K2_SCR + (u32)w * 8192 + (u32)pc * 1024);
                }
            } else if (w >= 4) {
                const char* wsrc = (const char*)wo + (size_t)(b >> 3) * 16384 + (w & 1) * 8192 + lane * 128;
                const char* xsrc = (const char*)x + (size_t)xcd * 262144 + (size_t)(b >> 3) * 8192 + lane * 128;
                touch4(w < 6 ? wsrc : xsrc, lds0 + K2_SINK);
                if (w == 7 && (b >> 3) < 2) CODE_PREFETCH(k3_oproj, k9_end, b >> 3, lds0 + K2_SINK);
            }
        } else if (it == 3) {
            asm volatile("s_waitcnt vmcnt(0)" ::: "memory");
            if (job) {
                const lds_cptr sp = (lds_cptr)lds + K2_SCR + w * 8192 + lane * 128;
                char* dst = dn_job ? ws + WS_GD + (size_t)(8 * jG) * 28672 + (size_t)(jc * 224 + 64 * jq + lane) * 16
                                   : ws + WS_GA + (size_t)(8 * jG) * 16384 + (size_t)((jq * 8 + jc) * 64 + lane) * 16;
                const u32 dpitch = dn_job ? 28672u : 16384u;
                const bool act = !(dn_job && jq == 3 && lane >= 32);
#pragma unroll 2
                for (int j = 0; j < 8; ++j) {
                    const u32x4 t = *(LAS const u32x4*)(sp + ((j ^ ((lane >> 1) & 7)) << 4));
                    if (act) st16<WT_REL>(dst + (size_t)j * dpitch, t);
                }
            }
        }
    }
    asm volatile("s_nop 1\n\tv_mfma_f32_32x32x16_bf16 %0, %1, %2, %0\n\ts_nop 7\n\ts_nop 7" : "+v"(o) : "v"(v1f), "v"(p1) : "memory");
    float l = o[8];
    bool bad = !(l < 1.0e18f);
#pragma unroll
    for (int i = 0; i < 8; ++i) bad |= !(fabsf(o[i]) < 3.0e38f);
    if (__any(bad)) {
        float m = -INFINITY;
        f32x16 z = {};
        o = z;
#pragma unroll 1
        for (int t = 0; t < 64; ++t) {
            f32x16 s = MFMA32(lds_frag(kp + t * 1024), qf, z);
            const float mn = fmaxf(m, half_max(max16(s)));
            const float alpha = __builtin_amdgcn_exp2f(m - mn);
            m = mn;
#pragma unroll
            for (int i = 0; i < 16; ++i) { o[i] *= alpha; s[i] -= mn; }
            u32x4 q0, q1;
            exp_pack(s, q0, q1);
            o = MFMA32(lds_frag(vp0 + t * K2_VSTRIDE), __builtin_bit_cast(bf16x8, q0), o);
            o = MFMA32(lds_frag(vp1 + t * K2_VSTRIDE), __builtin_bit_cast(bf16x8, q1), o);
        }
        l = o[8];
    }
    const float rl = 1.f / l;
    u32x4 ov = {pk_bf16(o[0] * rl, o[1] * rl), pk_bf16(o[2] * rl, o[3] * rl),
                pk_bf16(o[4] * rl, o[5] * rl), pk_bf16(o[6] * rl, o[7] * rl)};
    st16<WT_O>(ws + WS_O + ((size_t)(qt * 32 + head) * 64 + lane) * 16, ov);
}

constexpr int K3_LDS = 131072;
extern "C" __global__ __launch_bounds__(512, 2) void k3_oproj(const u16* __restrict__ x, const u16* __restrict__ wo,
                                                   const u16* __restrict__ wr, char* __restrict__ ws) {
    extern __shared__ __attribute__((aligned(16))) char lds[];
    const int tid = threadIdx.x, lane = tid & 63;
    const int w8 = __builtin_amdgcn_readfirstlane(tid >> 6);
    const int w = w8 & 3;
    const int b = blockIdx.x;
    const int rb = 4 * (b & 7) + ((b >> 3) & 3), cb = b >> 5;
    const u32 lds0 = (u32)(uintptr_t)lds;

    if (w8 >= 4) {
        const u32 aoff = (u32)lane * 16;
        const int r8 = lane >> 3, c8 = lane & 7;
        const u32 boff = (u32)r8 * 1024 + (u32)((c8 ^ (((lane >> 4) & 1) << 2)) * 16);
        const char* ab = ws + WS_O + (size_t)(rb * 2) * 32768;
        const char* bb = (const char*)wo + cb * 128;
        auto issue = [&](int c) {
#pragma unroll
            for (int i = 0; i < 2; ++i) {
                const int ao = i * 32768 + (4 * c + w) * 1024;
                glds16(aoff, ab + ao, lds0 + (u32)ao);
                const int pb = 8 * c + 2 * w + i;
                glds16(boff, bb + (size_t)pb * 8192, lds0 + 65536 + (u32)pb * 1024);
            }
        };
        issue(0); issue(1); issue(2);
#pragma unroll 1
        for (int c = 0; c < 5; ++c) { WAIT_BAR(8); issue(c + 3); }
        WAIT_BAR(8);
        WAIT_BAR(4);
        WAIT_BAR(0);
        return;
    }
    const int rt = w & 1, ct = w >> 1, r32 = lane & 31, h = lane >> 5;
    const int row = rb * 64 + rt * 32 + r32, col0 = cb * 64 + ct * 32 + 4 * h;
    u32x2 xr[4], wv[4];
#pragma unroll
    for (int g = 0; g < 4; ++g) {
        xr[g] = *(const u32x2*)(x + (size_t)row * 512 + col0 + 8 * g);
        wv[g] = *(const u32x2*)(wr + col0 + 8 * g);
    }
    const lds_cptr ap = (lds_cptr)lds + rt * 32768 + lane * 16;
    const int q4 = (lane & 15) >> 2, p4 = lane & 3, cg = (lane >> 4) & 1;
    const int wchunk = (4 * ct + 2 * cg + (p4 >> 1)) ^ ((q4 >> 1) << 2);
    u32 wa = lds0 + 65536 + (4 * h + q4) * 128 + wchunk * 16 + 8 * (p4 & 1);
    asm volatile("" : "+v"(wa));
    const lds_cptr wp = (lds_cptr)(uintptr_t)wa;

    f32x16 acc = {};
#pragma unroll
    for (int c = 0; c < 8; ++c) {
        asm volatile("s_barrier" ::: "memory");
#pragma unroll
        for (int i = 0; i < 4; ++i) {
            const int hd = 4 * c + i;
            bf16x8 of = lds_frag(ap + hd * 1024);
            bf16x8 wf = join_tr(lds_tr(wp + hd * 2048), lds_tr(wp + hd * 2048 + 1024));
            acc = MFMA32(wf, of, acc);
        }
    }
    float s1 = 0.f, s2 = 0.f, s3 = 0.f;
    u32 hw[4][2];
#pragma unroll
    for (int g = 0; g < 4; ++g) {
        float h0 = rbf(bflo2f(xr[g][0]) + rbf(acc[4 * g + 0]));
        float h1 = rbf(bfhi2f(xr[g][0]) + rbf(acc[4 * g + 1]));
        float h2 = rbf(bflo2f(xr[g][1]) + rbf(acc[4 * g + 2]));
        float h3 = rbf(bfhi2f(xr[g][1]) + rbf(acc[4 * g + 3]));
        s1 += (h0 + h1) + (h2 + h3);
        s2 += (h0 * h0 + h1 * h1) + (h2 * h2 + h3 * h3);
        s3 += (h0 * bflo2f(wv[g][0]) + h1 * bfhi2f(wv[g][0])) + (h2 * bflo2f(wv[g][1]) + h3 * bfhi2f(wv[g][1]));
        hw[g][0] = pk_bf16(h0, h1); hw[g][1] = pk_bf16(h2, h3);
    }
    u16* hp = (u16*)(ws + WS_H) + (size_t)row * 512 + cb * 64 + ct * 32 + 8 * h;
#pragma unroll
    for (int j = 0; j < 2; ++j) {
        auto r0 = __builtin_amdgcn_permlane32_swap(hw[2 * j][0], hw[2 * j + 1][0], false, false);
        auto r1 = __builtin_amdgcn_permlane32_swap(hw[2 * j][1], hw[2 * j + 1][1], false, false);
        st16<WT_H>(hp + 16 * j, u32x4{r0[0], r1[0], r0[1], r1[1]});
    }
    s1 = half_sum(s1); s2 = half_sum(s2); s3 = half_sum(s3);
    float* st = (float*)(ws + WS_ST);
    if (h == 0) {
        atomicAdd(st + row, s1);
        atomicAdd(st + 2048 + row, s2);
    } else {
        atomicAdd(st + 4096 + row, s3);
    }
}

struct LnPar { float mb, rden; };
__device__ __forceinline__ void ln_stats2(const u32x4& ra, const u32x4& rb, LnPar& pa, LnPar& pb) {
    float a1 = 0.f, a2 = 0.f, b1 = 0.f, b2 = 0.f;
#pragma unroll
    for (int i = 0; i < 4; ++i) {
        a1 = dot2(ra[i], 0x3f803f80u, a1); a2 = dot2(ra[i], ra[i], a2);
        b1 = dot2(rb[i], 0x3f803f80u, b1); b2 = dot2(rb[i], rb[i], b2);
    }
    a1 = wave_sum(a1); a2 = wave_sum(a2); b1 = wave_sum(b1); b2 = wave_sum(b2);
    const float ma = a1 * (1.f / 512.f), mb = b1 * (1.f / 512.f);
    const float va = fmaxf(a2 * (1.f / 512.f) - ma * ma, 0.f), vb = fmaxf(b2 * (1.f / 512.f) - mb * mb, 0.f);
    pa.mb = rbf(ma); pb.mb = rbf(mb);
    pa.rden = __builtin_amdgcn_rcpf(rbf(__builtin_amdgcn_sqrtf(rbf(rbf(va) + LN_EPS))));
    pb.rden = __builtin_amdgcn_rcpf(rbf(__builtin_amdgcn_sqrtf(rbf(rbf(vb) + LN_EPS))));
}
__device__ __forceinline__ float ln_y(float h, const LnPar& p) { return rbf(rbf(h - p.mb) * p.rden); }
__device__ __forceinline__ u32 route_key(float r, u32 idx) {
    u32 u = __float_as_uint(r);
    u = (u & 0x80000000u) ? ~u : (u | 0x80000000u);
    return (u & 0xfffff800u) | (2047u - idx);
}
template <int N> __device__ __forceinline__ void wave_reduce_scatter(float* v) {
#pragma unroll
    for (int i = 0; i < N / 2; ++i) {
        auto r = __builtin_amdgcn_permlane32_swap(__float_as_uint(v[i]), __float_as_uint(v[i + N / 2]), false, false);
        v[i] = __uint_as_float(r[0]) + __uint_as_float(r[1]);
    }
#pragma unroll
    for (int i = 0; i < N / 4; ++i) {
        auto r = __builtin_amdgcn_permlane16_swap(__float_as_uint(v[i]), __float_as_uint(v[i + N / 4]), false, false);
        v[i] = __uint_as_float(r[0]) + __uint_as_float(r[1]);
    }
#pragma unroll
    for (int i = 0; i < N / 4; ++i) v[i] = row16_sum(v[i]);
}

extern "C" __global__ __launch_bounds__(512, 2) void k4_tail(const u16* __restrict__ wr, char* __restrict__ ws, u16* __restrict__ out) {
    __shared__ __attribute__((aligned(16))) float sm[320];
    const int tid = threadIdx.x, lane = tid & 63;
    const int w8 = __builtin_amdgcn_readfirstlane(tid >> 6);
    const int w = w8 & 3, t4 = tid & 255;
    const int b = blockIdx.x;
    const int xcd = b & 7, jx = b >> 3;
    const int grp = xcd + 8 * (jx >> 3);
    const int ja = grp * 8 + (jx & 7), jb = xcd * 8 + (jx - 24);
    const bool isA = grp < 28, isB = jx >= 24;
    const u16* hbuf = (const u16*)(ws + WS_H);
    GAS u32* act = (GAS u32*)(ws + WS_GR);

    float wrf[8];
    const float* st = (const float*)(ws + WS_ST);
    const f32x4 s1a = *(const f32x4*)(st + tid * 4), s2a = *(const f32x4*)(st + 2048 + tid * 4), s3a = *(const f32x4*)(st + 4096 + tid * 4);
    const u32x4 wrv = *(const u32x4*)(wr + lane * 8);
    const char* ga = ws + WS_GA + ((isA && w8 < 4) ? (size_t)ja * 16384 + (size_t)(2 * w * 64 + lane) * 16 : (size_t)0);
    const u32x4 g0 = *(const u32x4*)(ga), g1 = *(const u32x4*)(ga + 1024);
    const u32x4 u0 = *(const u32x4*)(ga + 8192), u1 = *(const u32x4*)(ga + 8192 + 1024);
    {
#pragma unroll
        for (int i = 0; i < 4; ++i) { wrf[2 * i] = bflo2f(wrv[i]); wrf[2 * i + 1] = bfhi2f(wrv[i]); }
        const float wmean = wave_sum(((wrf[0] + wrf[1]) + (wrf[2] + wrf[3])) + ((wrf[4] + wrf[5]) + (wrf[6] + wrf[7]))) * (1.f / 512.f);
        u32 k1 = 0, k2 = 0;
#pragma unroll
        for (int i = 0; i < 4; ++i) {
            const float a1 = s1a[i], a2 = s2a[i], a3 = s3a[i];
            const float v512 = fmaxf(fmaf(a1 * (-1.f / 512.f), a1, a2), 0.f);
            const float r = fmaf(-a1, wmean, a3) * __builtin_amdgcn_rsqf(v512 + 512.f * LN_EPS);
            const u32 key = route_key(r, (u32)(tid * 4 + i));
            k2 = max(k2, min(k1, key)); k1 = max(k1, key);
        }
        const u32 t1 = wave_umax(k1);
        const u32 t2 = wave_umax(k1 == t1 ? k2 : k1);
        if (lane == 0) { ((u32*)sm)[288 + 2 * w8] = t1; ((u32*)sm)[288 + 2 * w8 + 1] = t2; }
    }
    LDS_BAR();
    int i1, i2;
    {
        const u32 key = ((u32*)sm)[288 + (lane & 15)];
        u32 t1 = max(key, dpp_u<0xB1>(key)); t1 = max(t1, dpp_u<0x4E>(t1)); t1 = max(t1, dpp_u<0x141>(t1)); t1 = max(t1, dpp_u<0x140>(t1));
        u32 t2 = key == t1 ? 0u : key;
        t2 = max(t2, dpp_u<0xB1>(t2)); t2 = max(t2, dpp_u<0x4E>(t2)); t2 = max(t2, dpp_u<0x141>(t2)); t2 = max(t2, dpp_u<0x140>(t2));
        i1 = __builtin_amdgcn_readfirstlane(2047 - (int)(t1 & 2047u));
        i2 = __builtin_amdgcn_readfirstlane(2047 - (int)(t2 & 2047u));
    }
    if (w8 < 4) {
        const u32x4 sel0 = *(const u32x4*)(hbuf + (size_t)i1 * 512 + lane * 8);
        const u32x4 sel1 = *(const u32x4*)(hbuf + (size_t)i2 * 512 + lane * 8);
        const int row0 = b * 8 + 2 * w;
        const u32x4 own0 = *(const u32x4*)(hbuf + (size_t)row0 * 512 + lane * 8);
        const u32x4 own1 = *(const u32x4*)(hbuf + (size_t)(row0 + 1) * 512 + lane * 8);
        LnPar pa, pb;
        ln_stats2(sel0, sel1, pa, pb);
        u32 h0 = sel0[0], h1 = sel1[0];
#pragma unroll
        for (int i = 1; i < 4; ++i) if (w == i) { h0 = sel0[i]; h1 = sel1[i]; }
        const float y00 = ln_y(bflo2f(h0), pa), y01 = ln_y(bfhi2f(h0), pa), y10 = ln_y(bflo2f(h1), pb), y11 = ln_y(bfhi2f(h1), pb);
        float v[32];
#pragma unroll
        for (int i = 0; i < 4; ++i) {
            const float ga_ = bflo2f(g0[i]), gb = bfhi2f(g0[i]), gc = bflo2f(g1[i]), gd = bfhi2f(g1[i]);
            const float ua = bflo2f(u0[i]), ub = bfhi2f(u0[i]), uc = bflo2f(u1[i]), ud = bfhi2f(u1[i]);
            v[2 * i] = y00 * ga_ + y01 * gc;     v[2 * i + 1] = y00 * gb + y01 * gd;
            v[8 + 2 * i] = y10 * ga_ + y11 * gc; v[8 + 2 * i + 1] = y10 * gb + y11 * gd;
            v[16 + 2 * i] = y00 * ua + y01 * uc; v[16 + 2 * i + 1] = y00 * ub + y01 * ud;
            v[24 + 2 * i] = y10 * ua + y11 * uc; v[24 + 2 * i + 1] = y10 * ub + y11 * ud;
        }
        wave_reduce_scatter<32>(v);
        if ((lane & 15) == 0) {
            *(f32x4*)(sm + 64 + w * 32 + (lane >> 4) * 8) = f32x4{v[0], v[1], v[2], v[3]};
            *(f32x4*)(sm + 64 + w * 32 + (lane >> 4) * 8 + 4) = f32x4{v[4], v[5], v[6], v[7]};
        }
        if (isB && w == 3) {
            float ha[8], hb[8], ya[8], yb[8], ra = 0.f, rb_ = 0.f;
#pragma unroll
            for (int i = 0; i < 4; ++i) { ha[2 * i] = bflo2f(sel0[i]); ha[2 * i + 1] = bfhi2f(sel0[i]); hb[2 * i] = bflo2f(sel1[i]); hb[2 * i + 1] = bfhi2f(sel1[i]); }
#pragma unroll
            for (int i = 0; i < 8; ++i) { ya[i] = ln_y(ha[i], pa); yb[i] = ln_y(hb[i], pb); ra += ya[i] * wrf[i]; rb_ += yb[i] * wrf[i]; }
            ra = rbf(wave_sum(ra)); rb_ = rbf(wave_sum(rb_));
            if (lane == jb) {
#pragma unroll
                for (int i = 0; i < 8; ++i) { sm[192 + i] = ha[i]; sm[200 + i] = hb[i]; sm[208 + i] = ya[i]; sm[216 + i] = yb[i]; }
                sm[16] = ra; sm[17] = rb_;
            }
        }
        LDS_BAR();
        if (isA && tid < 8) {
            float t[4];
#pragma unroll
            for (int j = 0; j < 4; ++j) t[j] = (sm[64 + 8 * j + tid] + sm[96 + 8 * j + tid]) + (sm[128 + 8 * j + tid] + sm[160 + 8 * j + tid]);
            float a[2];
#pragma unroll
            for (int k = 0; k < 2; ++k) {
                const float g = rbf(t[k]), u = rbf(t[2 + k]);
                const float sg = rbf(g * __builtin_amdgcn_rcpf(1.f + __expf(-g)));
                a[k] = sg * u;
            }
            u32 pv = pk_bf16(a[0], a[1]);
            if (pv == 0xffffffffu) pv = 0x7fc07fc0u;
            __hip_atomic_store(act + ja * 8 + tid, pv, __ATOMIC_RELAXED, __HIP_MEMORY_SCOPE_AGENT);
        }
        {
            LnPar po, pc;
            ln_stats2(own0, own1, po, pc);
            u32x4 oa, oc;
#pragma unroll
            for (int i = 0; i < 4; ++i) {
                const float a0 = bflo2f(own0[i]), a1 = bfhi2f(own0[i]), c0 = bflo2f(own1[i]), c1 = bfhi2f(own1[i]);
                oa[i] = pk_bf16(a0 + ln_y(a0, po), a1 + ln_y(a1, po));
                oc[i] = pk_bf16(c0 + ln_y(c0, pc), c1 + ln_y(c1, pc));
            }
            if (row0 != i1 && row0 != i2) st16<WT_OUT>(out + (size_t)row0 * 512 + lane * 8, oa);
            if (row0 + 1 != i1 && row0 + 1 != i2) st16<WT_OUT>(out + (size_t)(row0 + 1) * 512 + lane * 8, oc);
        }
        return;
    }

    __builtin_amdgcn_s_sleep(12);
    if (!isB) return;
    u32x4 dn[8];
    {
        const char* gd = ws + WS_GD + (size_t)jb * 28672 + (size_t)min(t4, 223) * 16;
#pragma unroll
        for (int i = 0; i < 8; ++i) dn[i] = *(const u32x4*)(gd + i * 3584);
    }
    LDS_BAR();
    if (isB) {
        typedef __attribute__((ext_vector_type(2))) float f32x2;
        f32x2 dw[8][4];
#pragma unroll
        for (int i = 0; i < 8; ++i)
#pragma unroll
            for (int c = 0; c < 4; ++c) dw[i][c] = f32x2{bflo2f(dn[i][c]), bfhi2f(dn[i][c])};
#pragma unroll
        for (int i = 0; i < 8; ++i) asm volatile("" : "+v"(dw[i][0]), "+v"(dw[i][1]), "+v"(dw[i][2]), "+v"(dw[i][3]));
        const int t16 = t4 & 15;
        float sig = rbf(__builtin_amdgcn_rcpf(1.f + __expf(-sm[16 + (t16 >> 3)])));
        float hv = sm[192 + t16], yv = sm[208 + t16];
        asm volatile("" : "+v"(sig), "+v"(hv), "+v"(yv));
        u32x4 av0 = {}, av1 = {};
        const GAS char* ap = (const GAS char*)act + min(t4, 223) * 32;
        for (u32 spins = 0; spins < (1u << 22); ++spins) {
            asm volatile("global_load_dwordx4 %0, %2, off sc1\n\tglobal_load_dwordx4 %1, %2, off offset:16 sc1\n\ts_waitcnt vmcnt(0)"
                         : "=&v"(av0), "=&v"(av1) : "v"(ap) : "memory");
            bool ok = true;
#pragma unroll
            for (int i = 0; i < 4; ++i) ok &= (av0[i] != 0xffffffffu) & (av1[i] != 0xffffffffu);
            if (__all(ok)) break;
        }
        f32x2 acc[8];
#pragma unroll
        for (int i = 0; i < 8; ++i) acc[i] = f32x2{0.f, 0.f};
        const u32 keep = t4 < 224 ? 0xffffffffu : 0u;
#pragma unroll
        for (int i = 0; i < 8; ++i) {
            const u32 aw = (i < 4 ? av0[i & 3] : av1[i & 3]) & keep;
            const float a0 = bflo2f(aw), a1 = bfhi2f(aw);
#pragma unroll
            for (int c = 0; c < 4; ++c) {
                acc[c] = __builtin_elementwise_fma(f32x2{a0, a0}, dw[i][c], acc[c]);
                acc[4 + c] = __builtin_elementwise_fma(f32x2{a1, a1}, dw[i][c], acc[4 + c]);
            }
        }
        float v[16];
#pragma unroll
        for (int i = 0; i < 8; ++i) { v[2 * i] = acc[i][0]; v[2 * i + 1] = acc[i][1]; }
        wave_reduce_scatter<16>(v);
        if ((lane & 15) == 0) *(f32x4*)(sm + 224 + w * 16 + (lane >> 4) * 4) = f32x4{v[0], v[1], v[2], v[3]};
        LDS_BAR();
        if (t4 < 16) {
            const float ysum = rbf((sm[224 + t4] + sm[240 + t4]) + (sm[256 + t4] + sm[272 + t4]));
            const float res = hv + rbf(yv + rbf(ysum * sig));
            const int orow = t4 < 8 ? i1 : i2;
            out[(size_t)orow * 512 + jb * 8 + (t4 & 7)] = (u16)(pk_bf16(res, 0.f) & 0xffffu);
        }
    }
}

extern "C" __global__ void k9_end() {}

extern "C" void kernel_launch(void* const* d_in, const int* in_sizes, int n_in,
                              void* d_out, int out_size, void* d_ws, size_t ws_size,
                              hipStream_t stream) {
    const u16* x = (const u16*)d_in[0];
    const u16* wqkv = (const u16*)d_in[1];
    const u16* wo = (const u16*)d_in[2];
    const u16* wr = (const u16*)d_in[4];
    const u16* wg = (const u16*)d_in[5];
    const u16* wu = (const u16*)d_in[6];
    const u16* wd = (const u16*)d_in[7];
    char* ws = (char*)d_ws;
    (void)hipFuncSetAttribute((const void*)k1_qkv, hipFuncAttributeMaxDynamicSharedMemorySize, K1_LDS);
    (void)hipFuncSetAttribute((const void*)k2_attn, hipFuncAttributeMaxDynamicSharedMemorySize, K2_LDS);
    (void)hipFuncSetAttribute((const void*)k3_oproj, hipFuncAttributeMaxDynamicSharedMemorySize, K3_LDS);
    k1_qkv<<<256, 512, K1_LDS, stream>>>(x, wqkv, ws);
    k2_attn<<<256, 512, K2_LDS, stream>>>(ws, x, wo, wg, wu, wd);
    k3_oproj<<<256, 512, K3_LDS, stream>>>(x, wo, wr, ws);
    k4_tail<<<256, 512, 0, stream>>>(wr, ws, (u16*)d_out);
}
```
